```python
import torch

def _softmax(x, axis=-1):
    return torch.softmax(x, dim=axis)
def _gelu(x):
    return torch.nn.functional.gelu(x, approximate="tanh")
def _silu(x):
    return torch.nn.functional.silu(x)
def _sigmoid(x):
    return torch.sigmoid(x)
def _layernorm(x, axis=-1, eps=1e-5):
    m = x.mean(dim=axis, keepdim=True)
    v = x.var(dim=axis, keepdim=True, unbiased=False)
    return (x - m) / torch.sqrt(v + eps)
def _scatter_add(tgt, idx, src, axis=0):
    return tgt.clone().index_add_(axis, idx, src)
def _segment_sum(src, seg, n):
    out = torch.zeros((n,) + src.shape[1:], dtype=src.dtype)
    return out.index_add_(0, seg, src)
def _pad(x, pw):
    flat = tuple(v for pair in reversed(pw) for v in pair)
    return torch.nn.functional.pad(x, flat)

kernel_name = 'concat_heads__gqa_block_cached__compressed_kv_cla__decode'

def setup_inputs():

    _w = (lambda a: (lambda t: t.to(torch.bfloat16) if t.is_floating_point() else t)(torch.as_tensor(a)))
    def _A_setup():
        B, Sq, D = 2, 1, 576
        Hq, Hkv, Sk, Dh = 32, 16, 4096, 128
        assert Hq % Hkv == 0, (Hq, Hkv)
        return {
            "x": _w((lambda *s: torch.randn(*s, dtype=torch.bfloat16) * 1.6)(B, Sq, D)),
            "k_cache": _w((lambda *s: torch.randn(*s, dtype=torch.bfloat16) * 1.6)(B, Hkv, Sk, Dh)),
            "v_cache": _w((lambda *s: torch.randn(*s, dtype=torch.bfloat16) * 0.05)(B, Hkv, Sk, Dh)),
            "wq": _w((lambda *s: torch.randn(*s, dtype=torch.bfloat16) * 1.6)(D, Hq * Dh) * (D ** -0.5)),
            "wo": _w((lambda *s: torch.randn(*s, dtype=torch.bfloat16) * 0.05)(Hq * Dh, D) * ((Hq * Dh) ** -0.5)),
        }
    _A_in = _A_setup()

    def _B_setup():
        B, H, Sq, Sk, D, M = 2, 32, 1, 4096, 128, 576
        Dc = 4 * D
        return {
            "x": _w((lambda *s: torch.randn(*s, dtype=torch.bfloat16) * 3.2)(B, Sq, M)),
            "c_kv": _w((lambda *s: torch.randn(*s, dtype=torch.bfloat16) * 0.1)(B, Sk, Dc)),
            "w_q": _w((lambda *s: torch.randn(*s, dtype=torch.bfloat16) * 6.4)(M, H, D) * (M ** -0.5)),
            "w_uk": _w((lambda *s: torch.randn(*s, dtype=torch.bfloat16) * 6.4)(Dc, H, D) * (Dc ** -0.5)),
            "w_uv": _w((lambda *s: torch.randn(*s, dtype=torch.bfloat16) * 0.1)(Dc, H, D) * (Dc ** -0.5)),
            "w_o": _w((lambda *s: torch.randn(*s, dtype=torch.bfloat16) * 0.1)(H, D, M) * ((H * D) ** -0.5)),
        }
    _B_in = _B_setup()

    return {
        "A_x": _A_in["x"],
        "A_k_cache": _A_in["k_cache"],
        "A_v_cache": _A_in["v_cache"],
        "A_wq": _A_in["wq"],
        "A_wo": _A_in["wo"],
        "B_x": _B_in["x"],
        "B_c_kv": _B_in["c_kv"],
        "B_w_q": _B_in["w_q"],
        "B_w_uk": _B_in["w_uk"],
        "B_w_uv": _B_in["w_uv"],
        "B_w_o": _B_in["w_o"],
    }

def reference(A_x, A_k_cache, A_v_cache, A_wq, A_wo, B_x, B_c_kv, B_w_q, B_w_uk, B_w_uv, B_w_o):
    def _A_ref(x, k_cache, v_cache, wq, wo):
        B, Sq, D = x.shape
        Hq, Dh = 32, 128
        Hkv = k_cache.shape[1]
        rep = Hq // Hkv
        q = (lambda x, *d: x.permute(*d))((x @ wq).reshape(B, Sq, Hq, Dh), 0, 2, 1, 3)
        k = (lambda x, n, axis: torch.repeat_interleave(x, n, dim=axis))(k_cache, rep, axis=1)
        v = (lambda x, n, axis: torch.repeat_interleave(x, n, dim=axis))(v_cache, rep, axis=1)
        scores = torch.einsum("bhqd,bhkd->bhqk", q, k) * (Dh ** -0.5)
        p = _softmax(scores, axis=-1)
        o = torch.einsum("bhqk,bhkd->bhqd", p, v)
        return (lambda x, *d: x.permute(*d))(o, 0, 2, 1, 3).reshape(B, Sq, Hq * Dh) @ wo

    def _B_ref(x, c_kv, w_q, w_uk, w_uv, w_o):
        B, Sq, M = x.shape[0], x.shape[1], x.shape[2]
        Sk = c_kv.shape[1]
        H, D = w_q.shape[1], w_q.shape[2]
        q = torch.einsum("bqm,mhd->bhqd", x, w_q)
        k = torch.einsum("bkc,chd->bhkd", c_kv, w_uk)
        v = torch.einsum("bkc,chd->bhkd", c_kv, w_uv)
        scores = torch.einsum("bhqd,bhkd->bhqk", q, k) * (D ** -0.5)
        off = Sk - Sq
        qi = torch.arange(Sq)[:, None]
        kj = torch.arange(Sk)[None, :]
        cmask = kj <= (qi + off)
        scores = torch.where(cmask[None, None], scores, -1e9)
        p = _softmax(scores, axis=-1)
        ctx = torch.einsum("bhqk,bhkd->bhqd", p, v)
        return torch.einsum("bhqd,hdm->bqm", ctx, w_o)

    a = _A_ref(A_x, A_k_cache, A_v_cache, A_wq, A_wo)
    b = _B_ref(B_x, B_c_kv, B_w_q, B_w_uk, B_w_uv, B_w_o)
    return torch.concatenate([a, b], axis=-1)
```

```cpp
#include <hip/hip_runtime.h>
#include <hip/hip_bf16.h>
#include <stdint.h>

typedef unsigned v4u __attribute__((ext_vector_type(4)));
typedef unsigned v2u __attribute__((ext_vector_type(2)));
typedef float f32x4 __attribute__((ext_vector_type(4)));
typedef short bf16x8 __attribute__((ext_vector_type(8)));

constexpr int DM = 576;
constexpr int SK = 4096;
constexpr int NSA = 6;
constexpr int NKS = 12;
constexpr int NSB = 32;
constexpr float LOG2E = 1.4426950408889634f;
constexpr float SCALE = 0.08838834764831845f;
constexpr float C2 = SCALE * LOG2E;

constexpr size_t WS_QA = 0;
constexpr size_t WS_QABS = WS_QA + (size_t)NKS * 2 * 4096;
constexpr size_t WS_PA = WS_QABS + (size_t)2 * 2 * 32 * 512;
constexpr size_t WS_PB = WS_PA + (size_t)32 * NSA * 2 * 132;
constexpr size_t WS_PBML = WS_PB + (size_t)2 * NSB * 32 * 512;
constexpr size_t WS_OA = WS_PBML + (size_t)2 * NSB * 32 * 2;
constexpr size_t WS_OB = WS_OA + (size_t)16 * 2 * DM;
constexpr size_t WS_END = WS_OB + (size_t)64 * 2 * DM;

__device__ __forceinline__ float bflo(unsigned u) { return __builtin_bit_cast(float, u << 16); }
__device__ __forceinline__ float bfhi(unsigned u) { return __builtin_bit_cast(float, u & 0xffff0000u); }
__device__ __forceinline__ unsigned cvtpk(float lo, float hi) {
  unsigned r; asm volatile("v_cvt_pk_bf16_f32 %0, %1, %2" : "=v"(r) : "v"(lo), "v"(hi)); return r;
}
__device__ __forceinline__ float rndbf(float x) { return bflo(cvtpk(x, x)); }
__device__ __forceinline__ f32x4 mfma16(v4u a, v4u b, f32x4 c) {
  return __builtin_amdgcn_mfma_f32_16x16x32_bf16(__builtin_bit_cast(bf16x8, a), __builtin_bit_cast(bf16x8, b), c, 0, 0, 0);
}
__device__ __forceinline__ v4u ldnt(const void* p) { return __builtin_nontemporal_load(reinterpret_cast<const v4u*>(p)); }
__device__ __forceinline__ v4u ld16(const void* p) { return *reinterpret_cast<const v4u*>(p); }
__device__ __forceinline__ float xg_max(float x) {
  auto s = __builtin_amdgcn_permlane16_swap(__float_as_uint(x), __float_as_uint(x), false, false);
  x = fmaxf(__uint_as_float(s[0]), __uint_as_float(s[1]));
  auto t = __builtin_amdgcn_permlane32_swap(__float_as_uint(x), __float_as_uint(x), false, false);
  return fmaxf(__uint_as_float(t[0]), __uint_as_float(t[1]));
}
__device__ __forceinline__ float xg_sum(float x) {
  auto s = __builtin_amdgcn_permlane16_swap(__float_as_uint(x), __float_as_uint(x), false, false);
  x = __uint_as_float(s[0]) + __uint_as_float(s[1]);
  auto t = __builtin_amdgcn_permlane32_swap(__float_as_uint(x), __float_as_uint(x), false, false);
  return __uint_as_float(t[0]) + __uint_as_float(t[1]);
}

struct Params {
  const uint16_t *xA, *kA, *vA, *wqA, *woA, *xB, *ckv, *wqB, *wuk, *wuv, *woB;
  uint16_t* out;
  float* ws;
};

__global__ void __launch_bounds__(256) k1(Params p) {
  __shared__ __attribute__((aligned(16))) float lds[32 * 2 * 64 + 2 * 64];
  const int t = threadIdx.x, bid = blockIdx.x;
  if (bid < 192) {
    const int kvh = bid / NKS, ks = bid % NKS;
    const int cg = t & 31, rg = t >> 5;
    v4u w[6];
#pragma unroll
    for (int i = 0; i < 6; ++i) w[i] = ldnt(p.wqA + (size_t)(48 * ks + 6 * rg + i) * 4096 + 256 * kvh + 8 * cg);
    float acc[2][8];
#pragma unroll
    for (int b = 0; b < 2; ++b)
#pragma unroll
      for (int j = 0; j < 8; ++j) acc[b][j] = 0.f;
#pragma unroll
    for (int i = 0; i < 6; ++i) {
      const float x0 = bflo(p.xA[48 * ks + 6 * rg + i]), x1 = bflo(p.xA[DM + 48 * ks + 6 * rg + i]);
#pragma unroll
      for (int j = 0; j < 4; ++j) {
        acc[0][2 * j] = fmaf(x0, bflo(w[i][j]), acc[0][2 * j]); acc[0][2 * j + 1] = fmaf(x0, bfhi(w[i][j]), acc[0][2 * j + 1]);
        acc[1][2 * j] = fmaf(x1, bflo(w[i][j]), acc[1][2 * j]); acc[1][2 * j + 1] = fmaf(x1, bfhi(w[i][j]), acc[1][2 * j + 1]);
      }
    }
#pragma unroll
    for (int b = 0; b < 2; ++b) {
      *reinterpret_cast<f32x4*>(&lds[(rg * 2 + b) * 256 + 8 * cg]) = f32x4{acc[b][0], acc[b][1], acc[b][2], acc[b][3]};
      *reinterpret_cast<f32x4*>(&lds[(rg * 2 + b) * 256 + 8 * cg + 4]) = f32x4{acc[b][4], acc[b][5], acc[b][6], acc[b][7]};
    }
    __syncthreads();
#pragma unroll
    for (int b = 0; b < 2; ++b) {
      float s = 0.f;
#pragma unroll
      for (int g = 0; g < 8; ++g) s += lds[(g * 2 + b) * 256 + t];
      p.ws[WS_QA + ((size_t)ks * 2 + b) * 4096 + 256 * kvh + t] = s;
    }
  } else {
    const int i0 = bid - 192, h = i0 >> 1, dh = i0 & 1;
    const int cg = t & 7, rg = t >> 3;
    float acc[2][8];
#pragma unroll
    for (int b = 0; b < 2; ++b)
#pragma unroll
      for (int j = 0; j < 8; ++j) acc[b][j] = 0.f;
    v4u w[18];
#pragma unroll
    for (int i = 0; i < 18; ++i) w[i] = ldnt(p.wqB + (size_t)(18 * rg + i) * 4096 + 128 * h + 64 * dh + 8 * cg);
    v4u uk[16];
#pragma unroll
    for (int i = 0; i < 16; ++i) uk[i] = ldnt(p.wuk + (size_t)(32 * i + rg) * 4096 + 128 * h + 64 * dh + 8 * cg);
#pragma unroll
    for (int i = 0; i < 18; ++i) {
      const float x0 = bflo(p.xB[18 * rg + i]), x1 = bflo(p.xB[DM + 18 * rg + i]);
#pragma unroll
      for (int j = 0; j < 4; ++j) {
        acc[0][2 * j] = fmaf(x0, bflo(w[i][j]), acc[0][2 * j]); acc[0][2 * j + 1] = fmaf(x0, bfhi(w[i][j]), acc[0][2 * j + 1]);
        acc[1][2 * j] = fmaf(x1, bflo(w[i][j]), acc[1][2 * j]); acc[1][2 * j + 1] = fmaf(x1, bfhi(w[i][j]), acc[1][2 * j + 1]);
      }
    }
#pragma unroll
    for (int b = 0; b < 2; ++b) {
      *reinterpret_cast<f32x4*>(&lds[(rg * 2 + b) * 64 + 8 * cg]) = f32x4{acc[b][0], acc[b][1], acc[b][2], acc[b][3]};
      *reinterpret_cast<f32x4*>(&lds[(rg * 2 + b) * 64 + 8 * cg + 4]) = f32x4{acc[b][4], acc[b][5], acc[b][6], acc[b][7]};
    }
    __syncthreads();
    float* q = lds + 32 * 2 * 64;
    if (t < 128) {
      float s = 0.f;
#pragma unroll
      for (int g = 0; g < 32; ++g) s += lds[g * 128 + t];
      q[t] = rndbf(s);
    }
    __syncthreads();
    float q0[8], q1[8];
#pragma unroll
    for (int j = 0; j < 8; ++j) { q0[j] = q[8 * cg + j]; q1[j] = q[64 + 8 * cg + j]; }
#pragma unroll
    for (int i = 0; i < 16; ++i) {
      float s0 = 0.f, s1 = 0.f;
#pragma unroll
      for (int j = 0; j < 4; ++j) {
        s0 = fmaf(q0[2 * j], bflo(uk[i][j]), s0); s0 = fmaf(q0[2 * j + 1], bfhi(uk[i][j]), s0);
        s1 = fmaf(q1[2 * j], bflo(uk[i][j]), s1); s1 = fmaf(q1[2 * j + 1], bfhi(uk[i][j]), s1);
      }
      s0 += __shfl_xor(s0, 1); s0 += __shfl_xor(s0, 2); s0 += __shfl_xor(s0, 4);
      s1 += __shfl_xor(s1, 1); s1 += __shfl_xor(s1, 2); s1 += __shfl_xor(s1, 4);
      const int c = 32 * i + rg;
      if (cg == 0) p.ws[WS_QABS + (((size_t)dh * 2 + 0) * 32 + h) * 512 + c] = s0;
      if (cg == 1) p.ws[WS_QABS + (((size_t)dh * 2 + 1) * 32 + h) * 512 + c] = s1;
    }
  }
}

__device__ __forceinline__ void attnA(const Params& p, int bid, float* lds) {
  const int t = threadIdx.x, lane = t & 63, w = __builtin_amdgcn_readfirstlane(t >> 6);
  const int pair = bid / NSA, s = bid % NSA, b = pair >> 4, kvh = pair & 15;
  const int t0 = (128 * s) / NSA, t1 = (128 * (s + 1)) / NSA;
  const int g = lane >> 4, c = lane & 15;
  const uint16_t* Kb = p.kA + (size_t)pair * SK * 128;
  const uint16_t* Vb = p.vA + (size_t)pair * SK * 128;
  v4u Kn[8], Vn[8];
  auto issue = [&](int tile) {
    const uint16_t* kp = Kb + (size_t)(32 * tile + c) * 128 + 8 * g;
    const uint16_t* vp = Vb + (size_t)(32 * tile + 4 * g) * 128 + 8 * c;
#pragma unroll
    for (int i = 0; i < 8; ++i) Kn[i] = ldnt(kp + (i >> 2) * 16 * 128 + (i & 3) * 32);
#pragma unroll
    for (int i = 0; i < 8; ++i) Vn[i] = ldnt(vp + (i >> 2) * 16 * 128 + (i & 3) * 128);
  };
  int tile = t0 + w;
  if (tile < t1) issue(tile);
  uint16_t* qs = reinterpret_cast<uint16_t*>(lds);
  if (t < 256) {
    float sacc = 0.f;
#pragma unroll
    for (int ks = 0; ks < NKS; ++ks) sacc += p.ws[WS_QA + ((size_t)ks * 2 + b) * 4096 + 256 * kvh + t];
    qs[t] = (uint16_t)(cvtpk(sacc, sacc) & 0xffffu);
  }
  __syncthreads();
  v4u qf[4];
#pragma unroll
  for (int ss = 0; ss < 4; ++ss) qf[ss] = ld16(qs + (c & 1) * 128 + 32 * ss + 8 * g);
  float m = -INFINITY, l = 0.f;
  f32x4 acc[8];
#pragma unroll
  for (int j = 0; j < 8; ++j) acc[j] = f32x4{0.f, 0.f, 0.f, 0.f};
  for (; tile < t1; tile += 8) {
    v4u Kc[8], Vc[8];
#pragma unroll
    for (int i = 0; i < 8; ++i) { Kc[i] = Kn[i]; Vc[i] = Vn[i]; }
    if (tile + 8 < t1) issue(tile + 8);
    f32x4 s0 = {0.f, 0.f, 0.f, 0.f}, s1 = {0.f, 0.f, 0.f, 0.f};
#pragma unroll
    for (int ss = 0; ss < 4; ++ss) { s0 = mfma16(Kc[ss], qf[ss], s0); s1 = mfma16(Kc[4 + ss], qf[ss], s1); }
    float tm = fmaxf(fmaxf(fmaxf(s0[0], s0[1]), fmaxf(s0[2], s0[3])), fmaxf(fmaxf(s1[0], s1[1]), fmaxf(s1[2], s1[3])));
    tm = xg_max(tm);
    const float mn = fmaxf(m, tm);
    const float alpha = __builtin_amdgcn_exp2f((m - mn) * C2);
    m = mn;
    float pv[8];
#pragma unroll
    for (int i = 0; i < 4; ++i) { pv[i] = __builtin_amdgcn_exp2f((s0[i] - mn) * C2); pv[4 + i] = __builtin_amdgcn_exp2f((s1[i] - mn) * C2); }
    v4u pf = {cvtpk(pv[0], pv[1]), cvtpk(pv[2], pv[3]), cvtpk(pv[4], pv[5]), cvtpk(pv[6], pv[7])};
    float ps = 0.f;
#pragma unroll
    for (int i = 0; i < 4; ++i) ps += bflo(pf[i]) + bfhi(pf[i]);
    l = fmaf(l, alpha, ps);
    const float a0 = __builtin_bit_cast(float, __builtin_amdgcn_readlane(__builtin_bit_cast(int, alpha), 0));
    const float a1 = __builtin_bit_cast(float, __builtin_amdgcn_readlane(__builtin_bit_cast(int, alpha), 1));
#pragma unroll
    for (int j = 0; j < 8; ++j) {
      v4u vf;
#pragma unroll
      for (int i = 0; i < 4; ++i) vf[i] = __builtin_amdgcn_perm(Vc[2 * i + 1][j >> 1], Vc[2 * i][j >> 1], (j & 1) ? 0x07060302u : 0x05040100u);
      acc[j][0] *= a0; acc[j][1] *= a1;
      acc[j] = mfma16(pf, vf, acc[j]);
    }
  }
  l = xg_sum(l);
  float* mrg = lds + 256;
  if (lane < 16) {
#pragma unroll
    for (int h = 0; h < 2; ++h) {
      float* rec = mrg + (w * 2 + h) * 132;
      *reinterpret_cast<f32x4*>(rec + 4 + 8 * c) = f32x4{acc[0][h], acc[1][h], acc[2][h], acc[3][h]};
      *reinterpret_cast<f32x4*>(rec + 4 + 8 * c + 4) = f32x4{acc[4][h], acc[5][h], acc[6][h], acc[7][h]};
      if (c == h) { rec[0] = m; rec[1] = l; }
    }
  }
  __syncthreads();
  if (t < 256) {
    const int h = t >> 7, d = t & 127;
    float M = -INFINITY;
#pragma unroll
    for (int ww = 0; ww < 8; ++ww) M = fmaxf(M, mrg[(ww * 2 + h) * 132]);
    float L = 0.f, o = 0.f;
#pragma unroll
    for (int ww = 0; ww < 8; ++ww) {
      const float* rec = mrg + (ww * 2 + h) * 132;
      const float wt = __builtin_amdgcn_exp2f((rec[0] - M) * C2);
      L = fmaf(wt, rec[1], L); o = fmaf(wt, rec[4 + d], o);
    }
    float* dst = p.ws + WS_PA + ((size_t)(pair * NSA + s) * 2 + h) * 132;
    dst[4 + d] = o;
    if (d == 0) { dst[0] = M; dst[1] = L; }
  }
}

__device__ __forceinline__ void attnB(const Params& p, int idx, float* lds) {
  const int t = threadIdx.x, lane = t & 63, w = __builtin_amdgcn_readfirstlane(t >> 6);
  const int b = idx >> 5, sp = idx & 31;
  const int g = lane >> 4, c = lane & 15;
  const uint16_t* Cb = p.ckv + ((size_t)b * SK + 128 * sp) * 512;
  v4u ca[16];
  {
    const uint16_t* cp = Cb + (size_t)(16 * w + c) * 512 + 8 * g;
#pragma unroll
    for (int i = 0; i < 16; ++i) ca[i] = ld16(cp + 32 * i);
  }
  uint16_t* qs = reinterpret_cast<uint16_t*>(lds);
  {
    const int head = t >> 4, ss = t & 15;
    const float* q0 = p.ws + WS_QABS + (((size_t)0 * 2 + b) * 32 + head) * 512 + 32 * ss;
    const float* q1 = p.ws + WS_QABS + (((size_t)1 * 2 + b) * 32 + head) * 512 + 32 * ss;
#pragma unroll
    for (int gg = 0; gg < 4; ++gg) {
      f32x4 a0 = *reinterpret_cast<const f32x4*>(q0 + 8 * gg), a1 = *reinterpret_cast<const f32x4*>(q0 + 8 * gg + 4);
      f32x4 b0 = *reinterpret_cast<const f32x4*>(q1 + 8 * gg), b1 = *reinterpret_cast<const f32x4*>(q1 + 8 * gg + 4);
      a0 += b0; a1 += b1;
      v4u o = {cvtpk(a0[0], a0[1]), cvtpk(a0[2], a0[3]), cvtpk(a1[0], a1[1]), cvtpk(a1[2], a1[3])};
      *reinterpret_cast<v4u*>(qs + ((((head >> 4) * 16 + ss) * 64) + gg * 16 + (head & 15)) * 8) = o;
    }
  }
  __syncthreads();
  f32x4 s0 = {0.f, 0.f, 0.f, 0.f}, s1 = {0.f, 0.f, 0.f, 0.f};
#pragma unroll
  for (int i = 0; i < 16; ++i) {
    s0 = mfma16(ca[i], ld16(qs + ((0 * 16 + i) * 64 + lane) * 8), s0);
    s1 = mfma16(ca[i], ld16(qs + ((1 * 16 + i) * 64 + lane) * 8), s1);
  }
  const int cb = w & 3, ht = w >> 2;
  v4u cv[32];
  {
    const uint16_t* cp = Cb + (size_t)(4 * g) * 512 + 128 * cb + 8 * c;
#pragma unroll
    for (int i = 0; i < 32; ++i) cv[i] = ld16(cp + (size_t)((i >> 2) * 16 + (i & 3)) * 512);
  }
  float* wmax = lds + 8192;
  float* wsum = wmax + 256;
  unsigned* pl = reinterpret_cast<unsigned*>(wsum + 256);
  float m0 = xg_max(fmaxf(fmaxf(s0[0], s0[1]), fmaxf(s0[2], s0[3])));
  float m1 = xg_max(fmaxf(fmaxf(s1[0], s1[1]), fmaxf(s1[2], s1[3])));
  if (lane < 16) { wmax[w * 32 + c] = m0; wmax[w * 32 + 16 + c] = m1; }
  __syncthreads();
#pragma unroll
  for (int ww = 0; ww < 8; ++ww) { m0 = fmaxf(m0, wmax[ww * 32 + c]); m1 = fmaxf(m1, wmax[ww * 32 + 16 + c]); }
  {
    unsigned a0 = cvtpk(__builtin_amdgcn_exp2f((s0[0] - m0) * C2), __builtin_amdgcn_exp2f((s0[1] - m0) * C2));
    unsigned a1 = cvtpk(__builtin_amdgcn_exp2f((s0[2] - m0) * C2), __builtin_amdgcn_exp2f((s0[3] - m0) * C2));
    unsigned b0 = cvtpk(__builtin_amdgcn_exp2f((s1[0] - m1) * C2), __builtin_amdgcn_exp2f((s1[1] - m1) * C2));
    unsigned b1 = cvtpk(__builtin_amdgcn_exp2f((s1[2] - m1) * C2), __builtin_amdgcn_exp2f((s1[3] - m1) * C2));
    float l0 = xg_sum(bflo(a0) + bfhi(a0) + bflo(a1) + bfhi(a1));
    float l1 = xg_sum(bflo(b0) + bfhi(b0) + bflo(b1) + bfhi(b1));
    if (lane < 16) { wsum[w * 32 + c] = l0; wsum[w * 32 + 16 + c] = l1; }
    const int kg = w >> 1, tau = w & 1;
    *reinterpret_cast<v2u*>(pl + ((0 * 4 + kg) * 64 + lane) * 4 + 2 * tau) = v2u{a0, a1};
    *reinterpret_cast<v2u*>(pl + ((1 * 4 + kg) * 64 + lane) * 4 + 2 * tau) = v2u{b0, b1};
  }
  __syncthreads();
  f32x4 acc[8];
#pragma unroll
  for (int j = 0; j < 8; ++j) acc[j] = f32x4{0.f, 0.f, 0.f, 0.f};
#pragma unroll
  for (int kg = 0; kg < 4; ++kg) {
    const v4u pf = *reinterpret_cast<const v4u*>(pl + ((ht * 4 + kg) * 64 + lane) * 4);
#pragma unroll
    for (int j = 0; j < 8; ++j) {
      v4u vf;
#pragma unroll
      for (int i = 0; i < 4; ++i) vf[i] = __builtin_amdgcn_perm(cv[8 * kg + 2 * i + 1][j >> 1], cv[8 * kg + 2 * i][j >> 1], (j & 1) ? 0x07060302u : 0x05040100u);
      acc[j] = mfma16(pf, vf, acc[j]);
    }
  }
  float* dst = p.ws + WS_PB + ((size_t)(b * NSB + sp) * 32 + 16 * ht + 4 * g) * 512 + 128 * cb + 8 * c;
#pragma unroll
  for (int r = 0; r < 4; ++r) {
    *reinterpret_cast<f32x4*>(dst + r * 512) = f32x4{acc[0][r], acc[1][r], acc[2][r], acc[3][r]};
    *reinterpret_cast<f32x4*>(dst + r * 512 + 4) = f32x4{acc[4][r], acc[5][r], acc[6][r], acc[7][r]};
  }
  if (t < 32) {
    float L = 0.f;
#pragma unroll
    for (int ww = 0; ww < 8; ++ww) L += wsum[ww * 32 + t];
    float M = -INFINITY;
#pragma unroll
    for (int ww = 0; ww < 8; ++ww) M = fmaxf(M, wmax[ww * 32 + t]);
    float* ml = p.ws + WS_PBML + ((size_t)(b * NSB + sp) * 32 + t) * 2;
    ml[0] = M; ml[1] = L;
  }
}

__global__ void __launch_bounds__(512) k2(Params p) {
  __shared__ __attribute__((aligned(16))) float lds[8192 + 512 + 2048];
  if (blockIdx.x < 192) attnA(p, blockIdx.x, lds);
  else attnB(p, blockIdx.x - 192, lds);
}

__global__ void __launch_bounds__(512) k3(Params p) {
  __shared__ __attribute__((aligned(16))) float lds[8 * 2 * 576 + 2048];
  const int t = threadIdx.x, lane = t & 63, w = t >> 6, bid = blockIdx.x;
  if (bid < 144) {
    const int kvh = bid / 9, cbk = bid % 9;
    const int cg = t & 7, rg = t >> 3;
    v4u wv[4];
#pragma unroll
    for (int i = 0; i < 4; ++i) wv[i] = ldnt(p.woA + (size_t)(256 * kvh + 4 * rg + i) * DM + 64 * cbk + 8 * cg);
    float* o = lds;
    {
      const int b = t >> 8, h = (t >> 7) & 1, d = t & 127;
      const float* src = p.ws + WS_PA + ((size_t)((b * 16 + kvh) * NSA) * 2 + h) * 132;
      float M = -INFINITY;
#pragma unroll
      for (int s = 0; s < NSA; ++s) M = fmaxf(M, src[s * 264]);
      float L = 0.f, ov = 0.f;
#pragma unroll
      for (int s = 0; s < NSA; ++s) {
        const float wt = __builtin_amdgcn_exp2f((src[s * 264] - M) * C2);
        L = fmaf(wt, src[s * 264 + 1], L); ov = fmaf(wt, src[s * 264 + 4 + d], ov);
      }
      o[t] = rndbf(ov / L);
    }
    __syncthreads();
    float acc[2][8];
#pragma unroll
    for (int b = 0; b < 2; ++b)
#pragma unroll
      for (int j = 0; j < 8; ++j) acc[b][j] = 0.f;
#pragma unroll
    for (int i = 0; i < 4; ++i) {
      const float x0 = o[4 * rg + i], x1 = o[256 + 4 * rg + i];
#pragma unroll
      for (int j = 0; j < 4; ++j) {
        acc[0][2 * j] = fmaf(x0, bflo(wv[i][j]), acc[0][2 * j]); acc[0][2 * j + 1] = fmaf(x0, bfhi(wv[i][j]), acc[0][2 * j + 1]);
        acc[1][2 * j] = fmaf(x1, bflo(wv[i][j]), acc[1][2 * j]); acc[1][2 * j + 1] = fmaf(x1, bfhi(wv[i][j]), acc[1][2 * j + 1]);
      }
    }
#pragma unroll
    for (int b = 0; b < 2; ++b)
#pragma unroll
      for (int j = 0; j < 8; ++j) {
        float v = acc[b][j];
        v += __shfl_xor(v, 8); v += __shfl_xor(v, 16); v += __shfl_xor(v, 32);
        acc[b][j] = v;
      }
    float* red = lds + 512;
    if (lane < 8) {
#pragma unroll
      for (int b = 0; b < 2; ++b)
#pragma unroll
        for (int j = 0; j < 8; ++j) red[(w * 2 + b) * 64 + 8 * cg + j] = acc[b][j];
    }
    __syncthreads();
    if (t < 128) {
      float s = 0.f;
#pragma unroll
      for (int ww = 0; ww < 8; ++ww) s += red[ww * 128 + t];
      p.ws[WS_OA + ((size_t)kvh * 2 + (t >> 6)) * DM + 64 * cbk + (t & 63)] = s;
    }
  } else {
    const int i0 = bid - 144, h = i0 >> 1, dh = i0 & 1;
    const int cg = t & 7, rg = t >> 3;
    v4u uv[8];
#pragma unroll
    for (int i = 0; i < 8; ++i) uv[i] = ldnt(p.wuv + (size_t)(8 * rg + i) * 4096 + 128 * h + 64 * dh + 8 * cg);
    float* ol = lds;
    float* wts = lds + 1024;
    if (t < 64) {
      const int b = t >> 5, s = t & 31;
      const float* ml = p.ws + WS_PBML + ((size_t)(b * NSB) * 32 + h) * 2;
      float M = -INFINITY;
      for (int ss = 0; ss < NSB; ++ss) M = fmaxf(M, ml[ss * 64]);
      float L = 0.f;
      for (int ss = 0; ss < NSB; ++ss) L += __builtin_amdgcn_exp2f((ml[ss * 64] - M) * C2) * ml[ss * 64 + 1];
      wts[t] = __builtin_amdgcn_exp2f((ml[s * 64] - M) * C2) / L;
    }
    __syncthreads();
#pragma unroll
    for (int b = 0; b < 2; ++b) {
      float s = 0.f;
      const float* src = p.ws + WS_PB + ((size_t)(b * NSB) * 32 + h) * 512 + t;
#pragma unroll 8
      for (int ss = 0; ss < NSB; ++ss) s = fmaf(wts[b * 32 + ss], src[(size_t)ss * 32 * 512], s);
      ol[b * 512 + t] = s;
    }
    __syncthreads();
    float acc[2][8];
#pragma unroll
    for (int b = 0; b < 2; ++b)
#pragma unroll
      for (int j = 0; j < 8; ++j) acc[b][j] = 0.f;
#pragma unroll
    for (int i = 0; i < 8; ++i) {
      const float x0 = ol[8 * rg + i], x1 = ol[512 + 8 * rg + i];
#pragma unroll
      for (int j = 0; j < 4; ++j) {
        acc[0][2 * j] = fmaf(x0, bflo(uv[i][j]), acc[0][2 * j]); acc[0][2 * j + 1] = fmaf(x0, bfhi(uv[i][j]), acc[0][2 * j + 1]);
        acc[1][2 * j] = fmaf(x1, bflo(uv[i][j]), acc[1][2 * j]); acc[1][2 * j + 1] = fmaf(x1, bfhi(uv[i][j]), acc[1][2 * j + 1]);
      }
    }
#pragma unroll
    for (int b = 0; b < 2; ++b)
#pragma unroll
      for (int j = 0; j < 8; ++j) {
        float v = acc[b][j];
        v += __shfl_xor(v, 8); v += __shfl_xor(v, 16); v += __shfl_xor(v, 32);
        acc[b][j] = v;
      }
    float* red = lds + 1024 + 64;
    if (lane < 8) {
#pragma unroll
      for (int b = 0; b < 2; ++b)
#pragma unroll
        for (int j = 0; j < 8; ++j) red[(w * 2 + b) * 64 + 8 * cg + j] = acc[b][j];
    }
    __syncthreads();
    float* ctx = lds;
    float cv = 0.f;
    if (t < 128) {
#pragma unroll
      for (int ww = 0; ww < 8; ++ww) cv += red[ww * 128 + t];
    }
    __syncthreads();
    if (t < 128) ctx[t] = rndbf(cv);
    __syncthreads();
    float* red2 = lds + 256;
    {
      float a2[2][8];
#pragma unroll
      for (int b = 0; b < 2; ++b)
#pragma unroll
        for (int j = 0; j < 8; ++j) a2[b][j] = 0.f;
      const int ch = t % 72, rgp = t / 72;
      if (rgp < 7) {
        for (int r = rgp; r < 64; r += 7) {
          const v4u wv = ldnt(p.woB + (size_t)(128 * h + 64 * dh + r) * DM + 8 * ch);
          const float x0 = ctx[r], x1 = ctx[64 + r];
#pragma unroll
          for (int j = 0; j < 4; ++j) {
            a2[0][2 * j] = fmaf(x0, bflo(wv[j]), a2[0][2 * j]); a2[0][2 * j + 1] = fmaf(x0, bfhi(wv[j]), a2[0][2 * j + 1]);
            a2[1][2 * j] = fmaf(x1, bflo(wv[j]), a2[1][2 * j]); a2[1][2 * j + 1] = fmaf(x1, bfhi(wv[j]), a2[1][2 * j + 1]);
          }
        }
#pragma unroll
        for (int b = 0; b < 2; ++b)
#pragma unroll
          for (int j = 0; j < 8; ++j) red2[(rgp * 2 + b) * DM + 8 * ch + j] = a2[b][j];
      }
    }
    __syncthreads();
    for (int i = t; i < 2 * DM; i += 512) {
      float s = 0.f;
#pragma unroll
      for (int gq = 0; gq < 7; ++gq) s += red2[gq * 2 * DM + i];
      p.ws[WS_OB + (size_t)i0 * 2 * DM + i] = s;
    }
  }
}

__global__ void __launch_bounds__(256) k4(Params p) {
  const int i = blockIdx.x * 256 + threadIdx.x;
  if (i >= 2 * 2 * DM) return;
  const int b = i / (2 * DM), r = i % (2 * DM);
  float s = 0.f;
  if (r < DM) {
#pragma unroll
    for (int k = 0; k < 16; ++k) s += p.ws[WS_OA + ((size_t)k * 2 + b) * DM + r];
  } else {
#pragma unroll 16
    for (int k = 0; k < 64; ++k) s += p.ws[WS_OB + ((size_t)k * 2 + b) * DM + (r - DM)];
  }
  p.out[i] = (uint16_t)(cvtpk(s, s) & 0xffffu);
}

extern "C" void kernel_launch(void* const* d_in, const int* in_sizes, int n_in, void* d_out, int out_size, void* d_ws, size_t ws_size,
                              hipStream_t stream) {
  Params p;
  p.xA = (const uint16_t*)d_in[0]; p.kA = (const uint16_t*)d_in[1]; p.vA = (const uint16_t*)d_in[2];
  p.wqA = (const uint16_t*)d_in[3]; p.woA = (const uint16_t*)d_in[4];
  p.xB = (const uint16_t*)d_in[5]; p.ckv = (const uint16_t*)d_in[6]; p.wqB = (const uint16_t*)d_in[7];
  p.wuk = (const uint16_t*)d_in[8]; p.wuv = (const uint16_t*)d_in[9]; p.woB = (const uint16_t*)d_in[10];
  p.out = (uint16_t*)d_out; p.ws = (float*)d_ws;
  k1<<<256, 256, 0, stream>>>(p);
  k2<<<256, 512, 0, stream>>>(p);
  k3<<<208, 512, 0, stream>>>(p);
  k4<<<9, 256, 0, stream>>>(p);
}
```

```cpp
#include <hip/hip_runtime.h>
#include <hip/hip_bf16.h>
#include <stdint.h>
#include <type_traits>

typedef unsigned v4u __attribute__((ext_vector_type(4)));
typedef unsigned v2u __attribute__((ext_vector_type(2)));
typedef float f32x4 __attribute__((ext_vector_type(4)));
typedef short bf16x8 __attribute__((ext_vector_type(8)));
typedef unsigned long long u64;
#define GAS __attribute__((address_space(1)))
typedef GAS u64 gu64;
#define RLX_AGENT __ATOMIC_RELAXED, __HIP_MEMORY_SCOPE_AGENT

constexpr int DM = 576;
constexpr int SK = 4096;
constexpr int NSA = 6;
constexpr int NSB = 32;
constexpr float LOG2E = 1.4426950408889634f;
constexpr float SCALE = 0.08838834764831845f;
constexpr float C2 = SCALE * LOG2E;

constexpr size_t WS_EA = 0;
constexpr size_t WS_EB = 256;
constexpr size_t WS_FB1 = 512;
constexpr size_t WS_FB2 = WS_FB1 + 512;
constexpr size_t WS_GQA = 2048;
constexpr size_t WS_GA = WS_GQA + (size_t)16 * 12 * 2 * 256 * 8;
constexpr size_t WS_GOA = WS_GA + (size_t)16 * 12 * 2 * 132 * 8;
constexpr size_t WS_GOB = WS_GOA + (size_t)12 * 16 * 96 * 8;
constexpr size_t WS_QABS = WS_GOB + (size_t)64 * 2 * DM * 8;
constexpr size_t WS_PB = WS_QABS + (size_t)2 * 2 * 32 * 512 * 2;
constexpr size_t WS_PBML = WS_PB + (size_t)2 * NSB * 32 * 512 * 2;
constexpr size_t WS_END = WS_PBML + (size_t)2 * NSB * 32 * 2 * 4;
constexpr size_t WS_DBG = 64u << 20;

#define STAMP(i) do {} while (0)

__device__ __forceinline__ float bflo(unsigned u) { return __uint_as_float(u << 16); }
__device__ __forceinline__ float bfhi(unsigned u) { return __uint_as_float(u & 0xffff0000u); }
typedef __bf16 bf16x2 __attribute__((ext_vector_type(2)));
typedef float f32x2 __attribute__((ext_vector_type(2)));
typedef short bf16x4 __attribute__((ext_vector_type(4)));
__device__ __forceinline__ unsigned cvtpk(float lo, float hi) {
  const bf16x2 r = __builtin_convertvector(f32x2{lo, hi}, bf16x2); return __builtin_bit_cast(unsigned, r);
}
__device__ __forceinline__ f32x4 mfma16k16(v2u a, v2u b, f32x4 c) {
  return __builtin_amdgcn_mfma_f32_16x16x16bf16_1k(__builtin_bit_cast(bf16x4, a), __builtin_bit_cast(bf16x4, b), c, 0, 0, 0);
}
#define SB() __builtin_amdgcn_sched_barrier(0)
#define WA 4
#define KQ 14
#define WB 8
__device__ __forceinline__ float rndbf(float x) { return bflo(cvtpk(x, x)); }
__device__ __forceinline__ f32x4 mfma16(v4u a, v4u b, f32x4 c) {
  return __builtin_amdgcn_mfma_f32_16x16x32_bf16(__builtin_bit_cast(bf16x8, a), __builtin_bit_cast(bf16x8, b), c, 0, 0, 0);
}
__device__ __forceinline__ v4u ldnt(const void* p) { return __builtin_nontemporal_load(reinterpret_cast<const v4u*>(p)); }
__device__ __forceinline__ v4u ld16(const void* p) { return *reinterpret_cast<const v4u*>(p); }
__device__ __forceinline__ float xg_max(float x) {
  auto s = __builtin_amdgcn_permlane16_swap(__float_as_uint(x), __float_as_uint(x), false, false);
  x = fmaxf(__uint_as_float(s[0]), __uint_as_float(s[1]));
  auto t = __builtin_amdgcn_permlane32_swap(__float_as_uint(x), __float_as_uint(x), false, false);
  return fmaxf(__uint_as_float(t[0]), __uint_as_float(t[1]));
}
__device__ __forceinline__ float x32_sum(float x) {
  auto t = __builtin_amdgcn_permlane32_swap(__float_as_uint(x), __float_as_uint(x), false, false);
  return __uint_as_float(t[0]) + __uint_as_float(t[1]);
}
__device__ __forceinline__ float xg_sum(float x) {
  auto s = __builtin_amdgcn_permlane16_swap(__float_as_uint(x), __float_as_uint(x), false, false);
  x = __uint_as_float(s[0]) + __uint_as_float(s[1]);
  return x32_sum(x);
}
template <int CTRL> __device__ __forceinline__ float dpp(float x) {
  return __builtin_bit_cast(float, __builtin_amdgcn_mov_dpp(__builtin_bit_cast(int, x), CTRL, 0xf, 0xf, true));
}
constexpr int XOR1 = 0xB1, XOR2 = 0x4E, XOR7 = 0x141, XOR8 = 0x128, ROR4 = 0x124;

__device__ __forceinline__ float q24(float v) { unsigned u = __float_as_uint(v); u += 0x7Fu + ((u >> 8) & 1u); return __uint_as_float(u & 0xffffff00u); }
__device__ __forceinline__ void st_gran(gu64* g, u64 tagw, float v) {
  unsigned u = __float_as_uint(v); u += 0x7Fu + ((u >> 8) & 1u);
  __hip_atomic_store(g, tagw | (u64)(u >> 8), RLX_AGENT);
}
__device__ __forceinline__ u64 ld_gran(gu64* g) { return __hip_atomic_load(g, RLX_AGENT); }
__device__ __forceinline__ float gval(u64 g) { return __uint_as_float((unsigned)g << 8); }
__device__ __forceinline__ bool gok(u64 g, u64 tagw) { return ((g ^ tagw) >> 24) == 0; }
constexpr unsigned SPIN_CAP = 1u << 22;
__device__ __forceinline__ void prepoll(gu64* g, u64 tagw) {
  for (unsigned spin = 0; spin < SPIN_CAP; ++spin) {
    if (__all(gok(ld_gran(g), tagw))) break;
    __builtin_amdgcn_s_sleep(2);
  }
}

struct Params {
  const uint16_t *xA, *kA, *vA, *wqA, *woA, *xB, *ckv, *wqB, *wuk, *wuv, *woB;
  uint16_t* out;
  unsigned char* ws;
};

__device__ __forceinline__ void roleA(const Params& p, int bid, float* lds) {
  const int t = threadIdx.x, lane = t & 63, w = __builtin_amdgcn_readfirstlane(t >> 6);
  const int kvh = bid / 12, j = bid % 12, b = j / NSA, s = j % NSA, pair = b * 16 + kvh;
  const int T0 = (256 * s) / NSA, T1 = (256 * (s + 1)) / NSA;
  const int nt = (T1 - T0 - w + 7) >> 3;
  const int g = lane >> 4, c = lane & 15;
  const uint16_t* Kb = p.kA + ((size_t)pair * SK + 16 * (T0 + w)) * 128 + 8 * lane;
  const uint16_t* Vb = p.vA + ((size_t)pair * SK + 16 * (T0 + w)) * 128 + 8 * lane;
  STAMP(0);
  v4u wq[3]; unsigned short x0[3], x1[3];
  {
    const int cg = t & 31, rg = t >> 5;
#pragma unroll
    for (int i = 0; i < 3; ++i) wq[i] = ldnt(p.wqA + (size_t)(48 * j + 3 * rg + i) * 4096 + 256 * kvh + 8 * cg);
#pragma unroll
    for (int i = 0; i < 3; ++i) { x0[i] = p.xA[48 * j + 3 * rg + i]; x1[i] = p.xA[DM + 48 * j + 3 * rg + i]; }
  }
  const u64 E = ld_gran((gu64*)(p.ws + WS_EA));
  unsigned char* ring = reinterpret_cast<unsigned char*>(lds) + w * 16384;
  v4u r[WA];
  auto issue = [&](int k) {
    const int n = k >> 3, i = k & 7;
    r[k % WA] = ldnt((i < 4 ? Kb : Vb) + (size_t)n * (8 * 16 * 128) + (i & 3) * 512);
  };
  auto put = [&](int k) {
    const int n = k >> 3, i = k & 7, row = 4 * (i & 3) + g;
    const int pos = i < 4 ? row * 256 + ((c ^ row) << 4) : 4096 + row * 256 + (c << 4);
    *reinterpret_cast<v4u*>(ring + (n & 1) * 8192 + pos) = r[k % WA];
  };
  SB();
#pragma unroll
  for (int k = 0; k < WA; ++k) issue(k);
  SB();
  const u64 tagw = (E + 1) << 24;
  float* scr = lds + 32768;
  {
    float acc[2][8];
#pragma unroll
    for (int bb = 0; bb < 2; ++bb)
#pragma unroll
      for (int jj = 0; jj < 8; ++jj) acc[bb][jj] = 0.f;
#pragma unroll
    for (int i = 0; i < 3; ++i) {
      const float a0 = bflo(x0[i]), a1 = bflo(x1[i]);
#pragma unroll
      for (int jj = 0; jj < 4; ++jj) {
        acc[0][2 * jj] = fmaf(a0, bflo(wq[i][jj]), acc[0][2 * jj]); acc[0][2 * jj + 1] = fmaf(a0, bfhi(wq[i][jj]), acc[0][2 * jj + 1]);
        acc[1][2 * jj] = fmaf(a1, bflo(wq[i][jj]), acc[1][2 * jj]); acc[1][2 * jj + 1] = fmaf(a1, bfhi(wq[i][jj]), acc[1][2 * jj + 1]);
      }
    }
#pragma unroll
    for (int bb = 0; bb < 2; ++bb)
#pragma unroll
      for (int jj = 0; jj < 8; ++jj) acc[bb][jj] = x32_sum(acc[bb][jj]);
    const int bb = lane >> 5, cg = lane & 31;
    f32x4 lo = bb ? f32x4{acc[1][0], acc[1][1], acc[1][2], acc[1][3]} : f32x4{acc[0][0], acc[0][1], acc[0][2], acc[0][3]};
    f32x4 hi = bb ? f32x4{acc[1][4], acc[1][5], acc[1][6], acc[1][7]} : f32x4{acc[0][4], acc[0][5], acc[0][6], acc[0][7]};
    *reinterpret_cast<f32x4*>(scr + (w * 2 + bb) * 256 + 8 * cg) = lo;
    *reinterpret_cast<f32x4*>(scr + (w * 2 + bb) * 256 + 8 * cg + 4) = hi;
  }
  __syncthreads();
  gu64* GQ = (gu64*)(p.ws + WS_GQA) + (size_t)kvh * 12 * 512;
  {
    float sacc = 0.f;
#pragma unroll
    for (int ww = 0; ww < 8; ++ww) sacc += scr[ww * 512 + t];
    st_gran(GQ + j * 512 + t, tagw, sacc);
  }
  STAMP(1);
  uint16_t* qs = reinterpret_cast<uint16_t*>(scr + 4096);
  gu64* qsrc = GQ + b * 256 + (t & 255);
  u64 gq[12];
  SB();
#pragma unroll
  for (int k = 0; k < 16; ++k) {
    put(k); SB(); issue(k + WA); SB();
    if (k == KQ && w >= 4) {
#pragma unroll
      for (int jj = 0; jj < 12; ++jj) gq[jj] = ld_gran(qsrc + jj * 512);
      SB();
    }
  }
  if (w >= 4) {
    for (unsigned spin = 0; spin < SPIN_CAP; ++spin) {
      bool ok = true;
#pragma unroll
      for (int jj = 0; jj < 12; ++jj) ok &= gok(gq[jj], tagw);
      if (__all(ok)) break;
      __builtin_amdgcn_s_sleep(1);
#pragma unroll
      for (int jj = 0; jj < 12; ++jj) gq[jj] = ld_gran(qsrc + jj * 512);
    }
    float sacc = 0.f;
#pragma unroll
    for (int jj = 0; jj < 12; ++jj) sacc += gval(gq[jj]);
    qs[t & 255] = (uint16_t)(cvtpk(sacc, sacc) & 0xffffu);
  }
  __syncthreads();
  STAMP(2);
  v4u qf[4];
#pragma unroll
  for (int ss = 0; ss < 4; ++ss) qf[ss] = ld16(qs + (c & 1) * 128 + 32 * ss + 8 * g);
  const int och = t % 6, org = t / 6;
  v4u wo[4];
  float m = -INFINITY, l = 0.f;
  f32x4 acc[8];
#pragma unroll
  for (int jj = 0; jj < 8; ++jj) acc[jj] = f32x4{0.f, 0.f, 0.f, 0.f};
  auto process = [&](int n) {
    const unsigned char* tb = ring + (n & 1) * 8192;
    f32x4 s0 = {0.f, 0.f, 0.f, 0.f};
#pragma unroll
    for (int ss = 0; ss < 4; ++ss) s0 = mfma16(*reinterpret_cast<const v4u*>(tb + c * 256 + (((4 * ss + g) ^ c) << 4)), qf[ss], s0);
    v4u vr[4];
#pragma unroll
    for (int i = 0; i < 4; ++i) vr[i] = *reinterpret_cast<const v4u*>(tb + 4096 + (4 * g + i) * 256 + (c << 4));
    const float tm = xg_max(fmaxf(fmaxf(s0[0], s0[1]), fmaxf(s0[2], s0[3])));
    const float mn = fmaxf(m, tm);
    const float alpha = __builtin_amdgcn_exp2f((m - mn) * C2);
    m = mn;
    const v2u pf = {cvtpk(__builtin_amdgcn_exp2f((s0[0] - mn) * C2), __builtin_amdgcn_exp2f((s0[1] - mn) * C2)),
                    cvtpk(__builtin_amdgcn_exp2f((s0[2] - mn) * C2), __builtin_amdgcn_exp2f((s0[3] - mn) * C2))};
    l = fmaf(l, alpha, (bflo(pf[0]) + bfhi(pf[0])) + (bflo(pf[1]) + bfhi(pf[1])));
    const float a0 = __builtin_bit_cast(float, __builtin_amdgcn_readlane(__builtin_bit_cast(int, alpha), 0));
    const float a1 = __builtin_bit_cast(float, __builtin_amdgcn_readlane(__builtin_bit_cast(int, alpha), 1));
#pragma unroll
    for (int jj = 0; jj < 8; ++jj) {
      const unsigned sel = (jj & 1) ? 0x07060302u : 0x05040100u;
      const v2u vf = {__builtin_amdgcn_perm(vr[1][jj >> 1], vr[0][jj >> 1], sel), __builtin_amdgcn_perm(vr[3][jj >> 1], vr[2][jj >> 1], sel)};
      acc[jj][0] *= a0; acc[jj][1] *= a1;
      acc[jj] = mfma16k16(pf, vf, acc[jj]);
    }
  };
  auto issue_wo = [&]() {
    if (t < 384) {
#pragma unroll
      for (int i = 0; i < 4; ++i) wo[i] = ldnt(p.woA + (size_t)(256 * kvh + 4 * org + i) * DM + 48 * j + 8 * och);
    }
  };
#pragma unroll
  for (int n = 0; n < 3; ++n) {
    SB(); process(n); SB();
#pragma unroll
    for (int i = 0; i < 8; ++i) {
      const int k = 8 * (n + 2) + i;
      put(k); SB();
      if (k + WA < 40) issue(k + WA);
      else if (nt == 6) issue(k + WA);
      else if (k + WA == 40) issue_wo();
      SB();
    }
  }
  process(3); SB();
  if (nt == 6) {
#pragma unroll
    for (int i = 0; i < 8; ++i) {
      const int k = 40 + i;
      put(k); SB();
      if (k + WA < 48) issue(k + WA);
      else if (k + WA == 48) issue_wo();
      SB();
    }
  }
  process(4);
  if (nt == 6) process(5);
  l = xg_sum(l);
  float* mrg = scr;
  if (lane < 16) {
#pragma unroll
    for (int h = 0; h < 2; ++h) {
      float* rec = mrg + (w * 2 + h) * 132;
      *reinterpret_cast<f32x4*>(rec + 4 + 8 * c) = f32x4{acc[0][h], acc[1][h], acc[2][h], acc[3][h]};
      *reinterpret_cast<f32x4*>(rec + 4 + 8 * c + 4) = f32x4{acc[4][h], acc[5][h], acc[6][h], acc[7][h]};
      if (c == h) { rec[0] = m; rec[1] = l; }
    }
  }
  __syncthreads();
  STAMP(3);
  gu64* GA = (gu64*)(p.ws + WS_GA) + (size_t)kvh * 12 * 2 * 132;
  if (t < 256) {
    const int h = t >> 7, d = t & 127;
    float M = -INFINITY;
#pragma unroll
    for (int ww = 0; ww < 8; ++ww) M = fmaxf(M, mrg[(ww * 2 + h) * 132]);
    M = q24(M);
    float L = 0.f, o = 0.f;
#pragma unroll
    for (int ww = 0; ww < 8; ++ww) {
      const float* rec = mrg + (ww * 2 + h) * 132;
      const float wt = __builtin_amdgcn_exp2f((rec[0] - M) * C2);
      L = fmaf(wt, rec[1], L); o = fmaf(wt, rec[4 + d], o);
    }
    gu64* dst = GA + (size_t)(j * 2 + h) * 132;
    st_gran(dst + 4 + d, tagw, o);
    if (d == 0) st_gran(dst, tagw, M);
    if (d == 1) st_gran(dst + 1, tagw, L);
  }
  float* os = scr + 8 * 2 * 132;
  {
    const int bb = t >> 8, h = (t >> 7) & 1, d = t & 127;
    gu64* src = GA + (size_t)(bb * NSA * 2 + h) * 132;
    prepoll(src + (lane < NSA ? lane : 0) * 264 + 4 + (d & 64), tagw);
    float Ms[NSA], Ls[NSA], Os[NSA];
    for (unsigned spin = 0; spin < SPIN_CAP; ++spin) {
      bool ok = true;
#pragma unroll
      for (int ss = 0; ss < NSA; ++ss) {
        const u64 a = ld_gran(src + ss * 264), bq = ld_gran(src + ss * 264 + 1), cq = ld_gran(src + ss * 264 + 4 + d);
        Ms[ss] = gval(a); Ls[ss] = gval(bq); Os[ss] = gval(cq);
        ok &= gok(a, tagw) && gok(bq, tagw) && gok(cq, tagw);
      }
      if (__all(ok)) break;
      __builtin_amdgcn_s_sleep(1);
    }
    float M = -INFINITY;
#pragma unroll
    for (int ss = 0; ss < NSA; ++ss) M = fmaxf(M, Ms[ss]);
    float L = 0.f, o = 0.f;
#pragma unroll
    for (int ss = 0; ss < NSA; ++ss) { const float wt = __builtin_amdgcn_exp2f((Ms[ss] - M) * C2); L = fmaf(wt, Ls[ss], L); o = fmaf(wt, Os[ss], o); }
    os[t] = rndbf(o / L);
  }
  __syncthreads();
  STAMP(4);
  float* red = lds;
  if (t < 384) {
    float a2[2][8];
#pragma unroll
    for (int bb = 0; bb < 2; ++bb)
#pragma unroll
      for (int jj = 0; jj < 8; ++jj) a2[bb][jj] = 0.f;
#pragma unroll
    for (int i = 0; i < 4; ++i) {
      const float y0 = os[4 * org + i], y1 = os[256 + 4 * org + i];
#pragma unroll
      for (int jj = 0; jj < 4; ++jj) {
        a2[0][2 * jj] = fmaf(y0, bflo(wo[i][jj]), a2[0][2 * jj]); a2[0][2 * jj + 1] = fmaf(y0, bfhi(wo[i][jj]), a2[0][2 * jj + 1]);
        a2[1][2 * jj] = fmaf(y1, bflo(wo[i][jj]), a2[1][2 * jj]); a2[1][2 * jj + 1] = fmaf(y1, bfhi(wo[i][jj]), a2[1][2 * jj + 1]);
      }
    }
#pragma unroll
    for (int bb = 0; bb < 2; ++bb) {
      *reinterpret_cast<f32x4*>(red + org * 96 + bb * 48 + 8 * och) = f32x4{a2[bb][0], a2[bb][1], a2[bb][2], a2[bb][3]};
      *reinterpret_cast<f32x4*>(red + org * 96 + bb * 48 + 8 * och + 4) = f32x4{a2[bb][4], a2[bb][5], a2[bb][6], a2[bb][7]};
    }
  }
  __syncthreads();
  float* red2 = scr;
  if (t < 384) {
    const int q4 = t / 96, col = t % 96;
    float sacc = 0.f;
#pragma unroll
    for (int i = 0; i < 16; ++i) sacc += red[(16 * q4 + i) * 96 + col];
    red2[q4 * 96 + col] = sacc;
  }
  __syncthreads();
  gu64* GO = (gu64*)(p.ws + WS_GOA) + (size_t)j * 16 * 96;
  if (t < 96) st_gran(GO + kvh * 96 + t, tagw, red2[t] + red2[96 + t] + red2[192 + t] + red2[288 + t]);
  STAMP(5);
  if (kvh == j) {
    __syncthreads();
    if (t < 384) {
      const int q4 = t / 96, col = t % 96;
      float v[4];
      for (unsigned spin = 0; spin < SPIN_CAP; ++spin) {
        bool ok = true;
#pragma unroll
        for (int i = 0; i < 4; ++i) { const u64 a = ld_gran(GO + (4 * q4 + i) * 96 + col); v[i] = gval(a); ok &= gok(a, tagw); }
        if (__all(ok)) break;
        __builtin_amdgcn_s_sleep(1);
      }
      red2[q4 * 96 + col] = (v[0] + v[1]) + (v[2] + v[3]);
    }
    __syncthreads();
    if (t < 96) {
      const float sacc = (red2[t] + red2[96 + t]) + (red2[192 + t] + red2[288 + t]);
      p.out[(t / 48) * 2 * DM + 48 * j + (t % 48)] = (uint16_t)(cvtpk(sacc, sacc) & 0xffffu);
    }
    if (j == 0 && t == 0) __hip_atomic_store((gu64*)(p.ws + WS_EA), E + 1, RLX_AGENT);
    STAMP(6);
  }
}

#define VMW(n) asm volatile("s_waitcnt vmcnt(" #n ")" ::: "memory")
#define VMWX(n) VMW(n)
__device__ __forceinline__ void roleB(const Params& p, int idx, float* lds) {
  const int t = threadIdx.x, lane = t & 63, w = __builtin_amdgcn_readfirstlane(t >> 6);
  const int g = lane >> 4, c = lane & 15;
  const __amdgpu_buffer_rsrc_t rs = __builtin_amdgcn_make_buffer_rsrc((void*)p.ws, 0, (int)WS_END, 0x00020000);
  STAMP(0);
  const int h1 = idx >> 1, dh = idx & 1;
  const int b2 = idx >> 5, sp = idx & 31;
  const int cg = t & 7, rg = t >> 3;
  unsigned char* cl = reinterpret_cast<unsigned char*>(lds);
  float* scr = lds + 32768;
  v4u wq[9], uk[8]; unsigned short x0[9], x1[9];
#pragma unroll
  for (int i = 0; i < 9; ++i) { x0[i] = p.xB[rg + 64 * i]; x1[i] = p.xB[DM + rg + 64 * i]; }
  const u64 E = ld_gran((gu64*)(p.ws + WS_EB));
#pragma unroll
  for (int i = 0; i < 9; ++i) wq[i] = ldnt(p.wqB + (size_t)(rg + 64 * i) * 4096 + 128 * h1 + 64 * dh + 8 * cg);
  SB();
  const u64 tagw = (E + 1) << 24;
  {
    float acc[2][8];
#pragma unroll
    for (int bb = 0; bb < 2; ++bb)
#pragma unroll
      for (int jj = 0; jj < 8; ++jj) acc[bb][jj] = 0.f;
#pragma unroll
    for (int i = 0; i < 9; ++i) {
      const float a0 = bflo(x0[i]), a1 = bflo(x1[i]);
#pragma unroll
      for (int jj = 0; jj < 4; ++jj) {
        acc[0][2 * jj] = fmaf(a0, bflo(wq[i][jj]), acc[0][2 * jj]); acc[0][2 * jj + 1] = fmaf(a0, bfhi(wq[i][jj]), acc[0][2 * jj + 1]);
        acc[1][2 * jj] = fmaf(a1, bflo(wq[i][jj]), acc[1][2 * jj]); acc[1][2 * jj + 1] = fmaf(a1, bfhi(wq[i][jj]), acc[1][2 * jj + 1]);
      }
      SB();
      if (i < 8) uk[i] = ldnt(p.wuk + (size_t)(8 * rg + i) * 4096 + 128 * h1 + 64 * dh + 8 * cg);
      SB();
    }
#pragma unroll
    for (int bb = 0; bb < 2; ++bb)
#pragma unroll
      for (int jj = 0; jj < 8; ++jj) { float v = acc[bb][jj]; v += dpp<XOR8>(v); acc[bb][jj] = xg_sum(v); }
    if (lane < 8) {
#pragma unroll
      for (int bb = 0; bb < 2; ++bb) {
        *reinterpret_cast<f32x4*>(scr + (w * 2 + bb) * 64 + 8 * cg) = f32x4{acc[bb][0], acc[bb][1], acc[bb][2], acc[bb][3]};
        *reinterpret_cast<f32x4*>(scr + (w * 2 + bb) * 64 + 8 * cg + 4) = f32x4{acc[bb][4], acc[bb][5], acc[bb][6], acc[bb][7]};
      }
    }
  }
  __syncthreads();
  float* qh = scr + 1024;
  if (t < 128) {
    float sacc = 0.f;
#pragma unroll
    for (int ww = 0; ww < 8; ++ww) sacc += scr[ww * 128 + t];
    qh[t] = rndbf(sacc);
  }
  __syncthreads();
  v4u ck[WB];
  const uint16_t* cb0 = p.ckv + ((size_t)b2 * SK + 128 * sp + 16 * w) * 512 + 8 * lane;
  v4u* qst = reinterpret_cast<v4u*>(scr + 2048);
  auto absorb = [&](auto PF) {
    float q0[8], q1[8];
#pragma unroll
    for (int jj = 0; jj < 8; ++jj) { q0[jj] = qh[8 * cg + jj]; q1[jj] = qh[64 + 8 * cg + jj]; }
    float v0[8], v1[8];
#pragma unroll
    for (int i = 0; i < 8; ++i) {
      float s0 = 0.f, s1 = 0.f;
#pragma unroll
      for (int jj = 0; jj < 4; ++jj) {
        s0 = fmaf(q0[2 * jj], bflo(uk[i][jj]), s0); s0 = fmaf(q0[2 * jj + 1], bfhi(uk[i][jj]), s0);
        s1 = fmaf(q1[2 * jj], bflo(uk[i][jj]), s1); s1 = fmaf(q1[2 * jj + 1], bfhi(uk[i][jj]), s1);
      }
      SB();
      if (PF.value && i < WB) ck[i] = ldnt(cb0 + i * 512);
      SB();
      s0 += dpp<XOR1>(s0); s0 += dpp<XOR2>(s0); s0 += dpp<XOR7>(s0);
      s1 += dpp<XOR1>(s1); s1 += dpp<XOR2>(s1); s1 += dpp<XOR7>(s1);
      v0[i] = s0; v1[i] = s1;
    }
    if (cg < 2) {
      v4u o;
      if (cg == 0) o = v4u{cvtpk(v0[0], v0[1]), cvtpk(v0[2], v0[3]), cvtpk(v0[4], v0[5]), cvtpk(v0[6], v0[7])};
      else o = v4u{cvtpk(v1[0], v1[1]), cvtpk(v1[2], v1[3]), cvtpk(v1[4], v1[5]), cvtpk(v1[6], v1[7])};
      qst[cg * 64 + rg] = o;
    }
  };
  if (w == 0) absorb(std::integral_constant<bool, false>{}); else absorb(std::integral_constant<bool, true>{});
  __syncthreads();
  if (w == 0) {
#pragma unroll
    for (int bb = 0; bb < 2; ++bb) {
      const int off = (int)WS_QABS + (((((dh * 2 + bb) * 2 + (h1 >> 4)) * 16 + (lane >> 2)) * 64) + (lane & 3) * 16 + (h1 & 15)) * 16;
      __builtin_amdgcn_raw_buffer_store_b128(qst[bb * 64 + lane], rs, off, 0, 16);
    }
    VMW(0);
    if (lane == 0) st_gran((gu64*)(p.ws + WS_FB1) + idx, tagw, 0.f);
    SB();
#pragma unroll
    for (int i = 0; i < WB; ++i) ck[i] = ldnt(cb0 + i * 512);
  }
  STAMP(1);
  SB();
#pragma unroll
  for (int i = 0; i < 16; ++i) {
    *reinterpret_cast<v4u*>(cl + (16 * w + i) * 1024 + ((lane ^ i) << 4)) = ck[i % WB];
    SB();
    if (i + WB < 16) ck[i % WB] = ldnt(cb0 + (i + WB) * 512);
    SB();
  }
  const int ht = w >> 2, kq = w & 3;
  prepoll((gu64*)(p.ws + WS_FB1) + lane, tagw);
  __builtin_amdgcn_fence(__ATOMIC_ACQUIRE, "wavefront");
  STAMP(2);
  v4u* ql = reinterpret_cast<v4u*>(scr);
  {
    v4u qa[2][4];
#pragma unroll
    for (int d2 = 0; d2 < 2; ++d2)
#pragma unroll
      for (int i = 0; i < 4; ++i)
        qa[d2][i] = __builtin_amdgcn_raw_buffer_load_b128(rs, (int)WS_QABS + ((d2 * 2 + b2) * 2048 + i * 512 + t) * 16, 0, 16);
    __syncthreads();
#pragma unroll
    for (int i = 0; i < 4; ++i) {
      v4u o;
#pragma unroll
      for (int jj = 0; jj < 4; ++jj) o[jj] = cvtpk(bflo(qa[0][i][jj]) + bflo(qa[1][i][jj]), bfhi(qa[0][i][jj]) + bfhi(qa[1][i][jj]));
      ql[i * 512 + t] = o;
    }
  }
  __syncthreads();
  float* wmax = scr;
  float* wsum = scr + 128;
  unsigned* pl = reinterpret_cast<unsigned*>(scr + 256);
  {
    f32x4 s0 = {0.f, 0.f, 0.f, 0.f}, s1 = {0.f, 0.f, 0.f, 0.f};
#pragma unroll
    for (int i = 0; i < 16; ++i) {
      const int ch = 4 * i + g;
      const int so = ((ch & ~15) | ((ch ^ c) & 15)) << 4;
      const v4u k0 = *reinterpret_cast<const v4u*>(cl + (32 * kq + c) * 1024 + so);
      const v4u k1 = *reinterpret_cast<const v4u*>(cl + (32 * kq + 16 + c) * 1024 + so);
      const v4u qf = ql[(ht * 16 + i) * 64 + lane];
      s0 = mfma16(k0, qf, s0); s1 = mfma16(k1, qf, s1);
    }
    float m0 = xg_max(fmaxf(fmaxf(fmaxf(s0[0], s0[1]), fmaxf(s0[2], s0[3])), fmaxf(fmaxf(s1[0], s1[1]), fmaxf(s1[2], s1[3]))));
    __syncthreads();
    if (lane < 16) wmax[kq * 32 + 16 * ht + c] = m0;
    __syncthreads();
#pragma unroll
    for (int q = 0; q < 4; ++q) m0 = fmaxf(m0, wmax[q * 32 + 16 * ht + c]);
    v4u pf;
    pf[0] = cvtpk(__builtin_amdgcn_exp2f((s0[0] - m0) * C2), __builtin_amdgcn_exp2f((s0[1] - m0) * C2));
    pf[1] = cvtpk(__builtin_amdgcn_exp2f((s0[2] - m0) * C2), __builtin_amdgcn_exp2f((s0[3] - m0) * C2));
    pf[2] = cvtpk(__builtin_amdgcn_exp2f((s1[0] - m0) * C2), __builtin_amdgcn_exp2f((s1[1] - m0) * C2));
    pf[3] = cvtpk(__builtin_amdgcn_exp2f((s1[2] - m0) * C2), __builtin_amdgcn_exp2f((s1[3] - m0) * C2));
    float l0 = 0.f;
#pragma unroll
    for (int i = 0; i < 4; ++i) l0 += bflo(pf[i]) + bfhi(pf[i]);
    l0 = xg_sum(l0);
    if (lane < 16) wsum[kq * 32 + 16 * ht + c] = l0;
    *reinterpret_cast<v4u*>(pl + ((ht * 4 + kq) * 64 + lane) * 4) = pf;
  }
  __syncthreads();
  {
    const int cb = w & 3;
    f32x4 acc[8];
#pragma unroll
    for (int jj = 0; jj < 8; ++jj) acc[jj] = f32x4{0.f, 0.f, 0.f, 0.f};
#pragma unroll
    for (int kg = 0; kg < 4; ++kg) {
      const v4u pf = *reinterpret_cast<const v4u*>(pl + ((ht * 4 + kg) * 64 + lane) * 4);
      v4u cv[8];
#pragma unroll
      for (int i = 0; i < 8; ++i) {
        const int key = 32 * kg + 16 * (i >> 2) + 4 * g + (i & 3);
        cv[i] = *reinterpret_cast<const v4u*>(cl + key * 1024 + ((16 * cb + (c ^ (key & 15))) << 4));
      }
#pragma unroll
      for (int jj = 0; jj < 8; ++jj) {
        v4u vf;
#pragma unroll
        for (int i = 0; i < 4; ++i) vf[i] = __builtin_amdgcn_perm(cv[2 * i + 1][jj >> 1], cv[2 * i][jj >> 1], (jj & 1) ? 0x07060302u : 0x05040100u);
        acc[jj] = mfma16(pf, vf, acc[jj]);
      }
    }
    const int off = (int)(WS_PB + (((size_t)(b2 * NSB + sp) * 32 + 16 * ht + 4 * g) * 512 + 128 * cb + 8 * c) * 2);
#pragma unroll
    for (int r = 0; r < 4; ++r) {
      v4u o = {cvtpk(acc[0][r], acc[1][r]), cvtpk(acc[2][r], acc[3][r]), cvtpk(acc[4][r], acc[5][r]), cvtpk(acc[6][r], acc[7][r])};
      __builtin_amdgcn_raw_buffer_store_b128(o, rs, off + r * 1024, 0, 16);
    }
    if (t < 32) {
      float L = 0.f, M = -INFINITY;
#pragma unroll
      for (int q = 0; q < 4; ++q) { L += wsum[q * 32 + t]; M = fmaxf(M, wmax[q * 32 + t]); }
      __builtin_amdgcn_raw_buffer_store_b64(v2u{__float_as_uint(M), __float_as_uint(L)}, rs, (int)(WS_PBML + ((size_t)(b2 * NSB + sp) * 32 + t) * 8), 0, 16);
    }
  }
  VMW(0);
  __syncthreads();
  if (t == 0) st_gran((gu64*)(p.ws + WS_FB2) + idx, tagw, 0.f);
  STAMP(3);
  v4u uv[8], wo[10];
  const int och = t % 72, org = t / 72;
#pragma unroll
  for (int i = 0; i < 8; ++i) uv[i] = ldnt(p.wuv + (size_t)(8 * rg + i) * 4096 + 128 * h1 + 64 * dh + 8 * cg);
#pragma unroll
  for (int i = 0; i < 10; ++i) {
    const int r = org + 7 * i;
    VMW(7);
    wo[i] = ldnt(p.woB + (size_t)(128 * h1 + 64 * dh + min(r, 63)) * DM + 8 * och);
  }
#pragma unroll
  for (int i = 0; i < 10; ++i) { const int r = org + 7 * i; if (r >= 64) wo[i] = v4u{0u, 0u, 0u, 0u}; }
  prepoll((gu64*)(p.ws + WS_FB2) + lane, tagw);
  __builtin_amdgcn_fence(__ATOMIC_ACQUIRE, "wavefront");
  STAMP(4);
  float* part = lds;
  float* ol = lds + 4096;
  {
    const int bb = t >> 8, sg = (t >> 6) & 3, cc = t & 63;
    v4u pv[8];
#pragma unroll
    for (int i = 0; i < 8; ++i)
      pv[i] = __builtin_amdgcn_raw_buffer_load_b128(rs, (int)(WS_PB + (((size_t)(bb * NSB + 8 * sg + i) * 32 + h1) * 512 + 8 * cc) * 2), 0, 16);
    const v2u ml = __builtin_amdgcn_raw_buffer_load_b64(rs, (int)(WS_PBML + ((size_t)(bb * NSB + (lane & 31)) * 32 + h1) * 8), 0, 16);
    const float mi = __uint_as_float(ml[0]), li = __uint_as_float(ml[1]);
    float M = mi;
    M = fmaxf(M, dpp<XOR1>(M)); M = fmaxf(M, dpp<XOR2>(M)); M = fmaxf(M, dpp<XOR7>(M)); M = fmaxf(M, dpp<XOR8>(M)); M = xg_max(M);
    const float e = __builtin_amdgcn_exp2f((mi - M) * C2);
    float L = e * li;
    L += dpp<XOR1>(L); L += dpp<XOR2>(L); L += dpp<XOR7>(L); L += dpp<XOR8>(L);
    { auto sw = __builtin_amdgcn_permlane16_swap(__float_as_uint(L), __float_as_uint(L), false, false); L = __uint_as_float(sw[0]) + __uint_as_float(sw[1]); }
    const float wl = e / L;
    float a[8];
#pragma unroll
    for (int jj = 0; jj < 8; ++jj) a[jj] = 0.f;
#pragma unroll
    for (int i = 0; i < 8; ++i) {
      const float wt = __builtin_bit_cast(float, __builtin_amdgcn_readlane(__builtin_bit_cast(int, wl), 8 * __builtin_amdgcn_readfirstlane(sg) + i));
#pragma unroll
      for (int jj = 0; jj < 4; ++jj) { a[2 * jj] = fmaf(wt, bflo(pv[i][jj]), a[2 * jj]); a[2 * jj + 1] = fmaf(wt, bfhi(pv[i][jj]), a[2 * jj + 1]); }
    }
    *reinterpret_cast<f32x4*>(part + (bb * 4 + sg) * 512 + 8 * cc) = f32x4{a[0], a[1], a[2], a[3]};
    *reinterpret_cast<f32x4*>(part + (bb * 4 + sg) * 512 + 8 * cc + 4) = f32x4{a[4], a[5], a[6], a[7]};
  }
  __syncthreads();
#pragma unroll
  for (int bb = 0; bb < 2; ++bb) ol[bb * 512 + t] = (part[(bb * 4) * 512 + t] + part[(bb * 4 + 1) * 512 + t]) + (part[(bb * 4 + 2) * 512 + t] + part[(bb * 4 + 3) * 512 + t]);
  __syncthreads();
  float* red = lds;
  {
    float acc[2][8];
#pragma unroll
    for (int bb = 0; bb < 2; ++bb)
#pragma unroll
      for (int jj = 0; jj < 8; ++jj) acc[bb][jj] = 0.f;
#pragma unroll
    for (int i = 0; i < 8; ++i) {
      const float y0 = ol[8 * rg + i], y1 = ol[512 + 8 * rg + i];
#pragma unroll
      for (int jj = 0; jj < 4; ++jj) {
        acc[0][2 * jj] = fmaf(y0, bflo(uv[i][jj]), acc[0][2 * jj]); acc[0][2 * jj + 1] = fmaf(y0, bfhi(uv[i][jj]), acc[0][2 * jj + 1]);
        acc[1][2 * jj] = fmaf(y1, bflo(uv[i][jj]), acc[1][2 * jj]); acc[1][2 * jj + 1] = fmaf(y1, bfhi(uv[i][jj]), acc[1][2 * jj + 1]);
      }
    }
#pragma unroll
    for (int bb = 0; bb < 2; ++bb)
#pragma unroll
      for (int jj = 0; jj < 8; ++jj) { float v = acc[bb][jj]; v += dpp<XOR8>(v); acc[bb][jj] = xg_sum(v); }
    if (lane < 8) {
#pragma unroll
      for (int bb = 0; bb < 2; ++bb)
#pragma unroll
        for (int jj = 0; jj < 8; ++jj) red[(w * 2 + bb) * 64 + 8 * cg + jj] = acc[bb][jj];
    }
  }
  __syncthreads();
  float* ctx = lds + 1024;
  if (t < 128) {
    float cvv = 0.f;
#pragma unroll
    for (int ww = 0; ww < 8; ++ww) cvv += red[ww * 128 + t];
    ctx[t] = rndbf(cvv);
  }
  __syncthreads();
  float* red2 = lds + 1280;
  if (org < 7) {
    float a2[2][8];
#pragma unroll
    for (int bb = 0; bb < 2; ++bb)
#pragma unroll
      for (int jj = 0; jj < 8; ++jj) a2[bb][jj] = 0.f;
#pragma unroll
    for (int i = 0; i < 10; ++i) {
      const int r = min(org + 7 * i, 63);
      const float y0 = ctx[r], y1 = ctx[64 + r];
#pragma unroll
      for (int jj = 0; jj < 4; ++jj) {
        a2[0][2 * jj] = fmaf(y0, bflo(wo[i][jj]), a2[0][2 * jj]); a2[0][2 * jj + 1] = fmaf(y0, bfhi(wo[i][jj]), a2[0][2 * jj + 1]);
        a2[1][2 * jj] = fmaf(y1, bflo(wo[i][jj]), a2[1][2 * jj]); a2[1][2 * jj + 1] = fmaf(y1, bfhi(wo[i][jj]), a2[1][2 * jj + 1]);
      }
    }
#pragma unroll
    for (int bb = 0; bb < 2; ++bb) {
      *reinterpret_cast<f32x4*>(red2 + (org * 2 + bb) * DM + 8 * och) = f32x4{a2[bb][0], a2[bb][1], a2[bb][2], a2[bb][3]};
      *reinterpret_cast<f32x4*>(red2 + (org * 2 + bb) * DM + 8 * och + 4) = f32x4{a2[bb][4], a2[bb][5], a2[bb][6], a2[bb][7]};
    }
  }
  __syncthreads();
  gu64* GOB = (gu64*)(p.ws + WS_GOB);
  for (int i = t; i < 2 * DM; i += 512) {
    float sacc = 0.f;
#pragma unroll
    for (int gq = 0; gq < 7; ++gq) sacc += red2[gq * 2 * DM + i];
    st_gran(GOB + (size_t)idx * 2 * DM + i, tagw, sacc);
  }
  STAMP(5);
  if (idx < 18) {
    const int bb = idx / 9, cbk = idx % 9;
    const int col = t & 63, sg = t >> 6;
    prepoll(GOB + ((size_t)(8 * sg + (lane & 7)) * 2 + bb) * DM + 64 * cbk, tagw);
    float v[8];
    for (unsigned spin = 0; spin < SPIN_CAP; ++spin) {
      bool ok = true;
#pragma unroll
      for (int i = 0; i < 8; ++i) { const u64 a = ld_gran(GOB + ((size_t)(8 * sg + i) * 2 + bb) * DM + 64 * cbk + col); v[i] = gval(a); ok &= gok(a, tagw); }
      if (__all(ok)) break;
      __builtin_amdgcn_s_sleep(1);
    }
    __syncthreads();
    lds[t] = ((v[0] + v[1]) + (v[2] + v[3])) + ((v[4] + v[5]) + (v[6] + v[7]));
    __syncthreads();
    if (t < 64) {
      float sacc = 0.f;
#pragma unroll
      for (int i = 0; i < 8; ++i) sacc += lds[64 * i + t];
      p.out[bb * 2 * DM + DM + 64 * cbk + t] = (uint16_t)(cvtpk(sacc, sacc) & 0xffffu);
    }
    if (idx == 0 && t == 0) __hip_atomic_store((gu64*)(p.ws + WS_EB), E + 1, RLX_AGENT);
    STAMP(6);
  }
}

__global__ void __launch_bounds__(512, 2) fused(Params p) {
  __shared__ __attribute__((aligned(16))) float lds[32768 + 8192];
  if (blockIdx.x < 192) roleA(p, blockIdx.x, lds);
  else roleB(p, blockIdx.x - 192, lds);
}

extern "C" void kernel_launch(void* const* d_in, const int* in_sizes, int n_in, void* d_out, int out_size, void* d_ws, size_t ws_size,
                              hipStream_t stream) {
  Params p;
  p.xA = (const uint16_t*)d_in[0]; p.kA = (const uint16_t*)d_in[1]; p.vA = (const uint16_t*)d_in[2];
  p.wqA = (const uint16_t*)d_in[3]; p.woA = (const uint16_t*)d_in[4];
  p.xB = (const uint16_t*)d_in[5]; p.ckv = (const uint16_t*)d_in[6]; p.wqB = (const uint16_t*)d_in[7];
  p.wuk = (const uint16_t*)d_in[8]; p.wuv = (const uint16_t*)d_in[9]; p.woB = (const uint16_t*)d_in[10];
  p.out = (uint16_t*)d_out; p.ws = (unsigned char*)d_ws;
  fused<<<256, 512, 0, stream>>>(p);
}
```

```cpp
#include <hip/hip_runtime.h>
#include <stdint.h>
#include <type_traits>

typedef unsigned v4u __attribute__((ext_vector_type(4)));
typedef unsigned v2u __attribute__((ext_vector_type(2)));
typedef float f32x4 __attribute__((ext_vector_type(4)));
typedef float f32x2 __attribute__((ext_vector_type(2)));
typedef short bf16x8 __attribute__((ext_vector_type(8)));
typedef short bf16x4 __attribute__((ext_vector_type(4)));
typedef __bf16 bf16x2 __attribute__((ext_vector_type(2)));
typedef unsigned long long u64;
#define GAS __attribute__((address_space(1)))
typedef GAS u64 gu64;
#define RLX_AGENT __ATOMIC_RELAXED, __HIP_MEMORY_SCOPE_AGENT

constexpr int DM = 576;
constexpr int SK = 4096;
constexpr int NSA = 4;
constexpr int NSB = 32;
constexpr float LOG2E = 1.4426950408889634f;
constexpr float SCALE = 0.08838834764831845f;
constexpr float C2 = SCALE * LOG2E;

#define WA 8
#define KQ 12
#define WB 8
#define ADELAY 0
#define XALL 1

constexpr size_t WS_E = 0;
constexpr size_t WS_FB1 = 512;
constexpr size_t WS_FB2 = WS_FB1 + 512;
constexpr size_t WS_GQA = 2048;
constexpr size_t WS_GA = WS_GQA + (size_t)16 * 2 * 256 * 8;
constexpr size_t WS_GOA = WS_GA + (size_t)16 * 2 * NSA * 2 * 132 * 8;
constexpr size_t WS_GOB = WS_GOA + (size_t)4 * 16 * 288 * 8;
constexpr size_t WS_QABS = WS_GOB + (size_t)64 * 2 * DM * 8;
constexpr size_t WS_PB = WS_QABS + (size_t)2 * 2 * 32 * 512 * 2;
constexpr size_t WS_PBML = WS_PB + (size_t)2 * NSB * 32 * 512 * 2;
constexpr size_t WS_END = WS_PBML + (size_t)2 * NSB * 32 * 2 * 4;
constexpr size_t WS_DBG = 64u << 20;

#define STAMP(i) do {} while (0)
#define SB() __builtin_amdgcn_sched_barrier(0)
#define VMW(n) asm volatile("s_waitcnt vmcnt(" #n ")" ::: "memory")

__device__ __forceinline__ float bflo(unsigned u) { return __uint_as_float(u << 16); }
__device__ __forceinline__ float bfhi(unsigned u) { return __uint_as_float(u & 0xffff0000u); }
__device__ __forceinline__ unsigned cvtpk(float lo, float hi) {
  const bf16x2 r = __builtin_convertvector(f32x2{lo, hi}, bf16x2); return __builtin_bit_cast(unsigned, r);
}
__device__ __forceinline__ float rndbf(float x) { return bflo(cvtpk(x, x)); }
__device__ __forceinline__ f32x4 mfma16(v4u a, v4u b, f32x4 c) {
  return __builtin_amdgcn_mfma_f32_16x16x32_bf16(__builtin_bit_cast(bf16x8, a), __builtin_bit_cast(bf16x8, b), c, 0, 0, 0);
}
__device__ __forceinline__ f32x4 mfma16k16(v2u a, v2u b, f32x4 c) {
  return __builtin_amdgcn_mfma_f32_16x16x16bf16_1k(__builtin_bit_cast(bf16x4, a), __builtin_bit_cast(bf16x4, b), c, 0, 0, 0);
}
__device__ __forceinline__ v4u ldnt(const void* p) { return __builtin_nontemporal_load(reinterpret_cast<const v4u*>(p)); }
__device__ __forceinline__ v4u ld16(const void* p) { return *reinterpret_cast<const v4u*>(p); }
__device__ __forceinline__ float xg_max(float x) {
  auto s = __builtin_amdgcn_permlane16_swap(__float_as_uint(x), __float_as_uint(x), false, false);
  x = fmaxf(__uint_as_float(s[0]), __uint_as_float(s[1]));
  auto t = __builtin_amdgcn_permlane32_swap(__float_as_uint(x), __float_as_uint(x), false, false);
  return fmaxf(__uint_as_float(t[0]), __uint_as_float(t[1]));
}
__device__ __forceinline__ float xg_sum(float x) {
  auto s = __builtin_amdgcn_permlane16_swap(__float_as_uint(x), __float_as_uint(x), false, false);
  x = __uint_as_float(s[0]) + __uint_as_float(s[1]);
  auto t = __builtin_amdgcn_permlane32_swap(__float_as_uint(x), __float_as_uint(x), false, false);
  return __uint_as_float(t[0]) + __uint_as_float(t[1]);
}
template <int CTRL> __device__ __forceinline__ float dpp(float x) {
  return __builtin_bit_cast(float, __builtin_amdgcn_mov_dpp(__builtin_bit_cast(int, x), CTRL, 0xf, 0xf, true));
}
constexpr int XOR1 = 0xB1, XOR2 = 0x4E, XOR7 = 0x141, XOR8 = 0x128;

__device__ __forceinline__ float q24(float v) { unsigned u = __float_as_uint(v); u += 0x7Fu + ((u >> 8) & 1u); return __uint_as_float(u & 0xffffff00u); }
__device__ __forceinline__ void st_gran(gu64* g, u64 tagw, float v) {
  unsigned u = __float_as_uint(v); u += 0x7Fu + ((u >> 8) & 1u);
  __hip_atomic_store(g, tagw | (u64)(u >> 8), RLX_AGENT);
}
__device__ __forceinline__ u64 ld_gran(gu64* g) { return __hip_atomic_load(g, RLX_AGENT); }
__device__ __forceinline__ float gval(u64 g) { return __uint_as_float((unsigned)g << 8); }
__device__ __forceinline__ bool gok(u64 g, u64 tagw) { return ((g ^ tagw) >> 24) == 0; }
constexpr unsigned SPIN_CAP = 1u << 22;
__device__ __forceinline__ void prepoll(gu64* g, u64 tagw) {
  for (unsigned spin = 0; spin < SPIN_CAP; ++spin) {
    if (__all(gok(ld_gran(g), tagw))) break;
    __builtin_amdgcn_s_sleep(2);
  }
}
__device__ __forceinline__ void fma8(float (&acc)[2][8], float a0, float a1, v4u w) {
#pragma unroll
  for (int jj = 0; jj < 4; ++jj) {
    acc[0][2 * jj] = fmaf(a0, bflo(w[jj]), acc[0][2 * jj]); acc[0][2 * jj + 1] = fmaf(a0, bfhi(w[jj]), acc[0][2 * jj + 1]);
    acc[1][2 * jj] = fmaf(a1, bflo(w[jj]), acc[1][2 * jj]); acc[1][2 * jj + 1] = fmaf(a1, bfhi(w[jj]), acc[1][2 * jj + 1]);
  }
}
__device__ __forceinline__ void zero8(float (&acc)[2][8]) {
#pragma unroll
  for (int bb = 0; bb < 2; ++bb)
#pragma unroll
    for (int jj = 0; jj < 8; ++jj) acc[bb][jj] = 0.f;
}

struct Params {
  const uint16_t *xA, *kA, *vA, *wqA, *woA, *xB, *ckv, *wqB, *wuk, *wuv, *woB;
  uint16_t* out;
  unsigned char* ws;
};

__device__ __forceinline__ void roleA(const Params& p, int bid, float* lds) {
  const int t = threadIdx.x, lane = t & 63, w = __builtin_amdgcn_readfirstlane(t >> 6);
  const int kvh = bid >> 3, b = (bid >> 2) & 1, s = bid & 3, pair = b * 16 + kvh;
  constexpr int NT = 8, NK = 8 * NT;
  const int g = lane >> 4, c = lane & 15;
  const uint16_t* Kb = p.kA + ((size_t)pair * SK + 16 * (64 * s + w)) * 128 + 8 * lane;
  const uint16_t* Vb = p.vA + ((size_t)pair * SK + 16 * (64 * s + w)) * 128 + 8 * lane;
  STAMP(0);
  const u64 E = ld_gran((gu64*)(p.ws + WS_E));
  if (ADELAY > 0) {
    const u64 tt0 = __builtin_amdgcn_s_memrealtime();
    while (__builtin_amdgcn_s_memrealtime() - tt0 < (u64)ADELAY) __builtin_amdgcn_s_sleep(4);
  }
  unsigned char* ring = reinterpret_cast<unsigned char*>(lds) + w * 16384;
  v4u r[WA];
  auto issue = [&](int k) {
    const int n = k >> 3, i = k & 7;
    r[k % WA] = ldnt((i < 4 ? Kb : Vb) + (size_t)n * (8 * 16 * 128) + (i & 3) * 512);
  };
  auto put = [&](int k) {
    const int n = k >> 3, i = k & 7, row = 4 * (i & 3) + g;
    const int pos = i < 4 ? row * 256 + ((c ^ row) << 4) : 4096 + row * 256 + (c << 4);
    *reinterpret_cast<v4u*>(ring + (n & 1) * 8192 + pos) = r[k % WA];
  };
  SB();
#pragma unroll
  for (int k = 0; k < WA; ++k) issue(k);
  SB();
  const u64 tagw = (E + 1) << 24;
  float* scr = lds + 32768;
  uint16_t* qs = reinterpret_cast<uint16_t*>(scr + 4096);
  gu64* qsrc = (gu64*)(p.ws + WS_GQA) + (size_t)pair * 0 + (size_t)(kvh * 2 + b) * 256 + (t & 255);
  u64 gq = 0;
#pragma unroll
  for (int k = 0; k < 16; ++k) {
    put(k); SB(); issue(k + WA); SB();
    if (k == KQ) { gq = ld_gran(qsrc); SB(); }
  }
  if (w >= 4) {
    for (unsigned spin = 0; spin < SPIN_CAP; ++spin) {
      if (__all(gok(gq, tagw))) break;
      __builtin_amdgcn_s_sleep(1);
      gq = ld_gran(qsrc);
    }
    qs[t & 255] = (uint16_t)((unsigned)gq >> 8);
  }
  __syncthreads();
  STAMP(1);
  v4u qf[4];
#pragma unroll
  for (int ss = 0; ss < 4; ++ss) qf[ss] = ld16(qs + (c & 1) * 128 + 32 * ss + 8 * g);
  float m = -INFINITY, l = 0.f;
  f32x4 acc[8];
#pragma unroll
  for (int jj = 0; jj < 8; ++jj) acc[jj] = f32x4{0.f, 0.f, 0.f, 0.f};
  auto process = [&](int n) {
    const unsigned char* tb = ring + (n & 1) * 8192;
    f32x4 s0 = {0.f, 0.f, 0.f, 0.f};
#pragma unroll
    for (int ss = 0; ss < 4; ++ss) s0 = mfma16(*reinterpret_cast<const v4u*>(tb + c * 256 + (((4 * ss + g) ^ c) << 4)), qf[ss], s0);
    v4u vr[4];
#pragma unroll
    for (int i = 0; i < 4; ++i) vr[i] = *reinterpret_cast<const v4u*>(tb + 4096 + (4 * g + i) * 256 + (c << 4));
    const float tm = xg_max(fmaxf(fmaxf(s0[0], s0[1]), fmaxf(s0[2], s0[3])));
    const float mn = fmaxf(m, tm);
    const float alpha = __builtin_amdgcn_exp2f((m - mn) * C2);
    m = mn;
    const v2u pf = {cvtpk(__builtin_amdgcn_exp2f((s0[0] - mn) * C2), __builtin_amdgcn_exp2f((s0[1] - mn) * C2)),
                    cvtpk(__builtin_amdgcn_exp2f((s0[2] - mn) * C2), __builtin_amdgcn_exp2f((s0[3] - mn) * C2))};
    l = fmaf(l, alpha, (bflo(pf[0]) + bfhi(pf[0])) + (bflo(pf[1]) + bfhi(pf[1])));
    const float a0 = __builtin_bit_cast(float, __builtin_amdgcn_readlane(__builtin_bit_cast(int, alpha), 0));
    const float a1 = __builtin_bit_cast(float, __builtin_amdgcn_readlane(__builtin_bit_cast(int, alpha), 1));
#pragma unroll
    for (int jj = 0; jj < 8; ++jj) {
      const unsigned sel = (jj & 1) ? 0x07060302u : 0x05040100u;
      const v2u vf = {__builtin_amdgcn_perm(vr[1][jj >> 1], vr[0][jj >> 1], sel), __builtin_amdgcn_perm(vr[3][jj >> 1], vr[2][jj >> 1], sel)};
      acc[jj][0] *= a0; acc[jj][1] *= a1;
      acc[jj] = mfma16k16(pf, vf, acc[jj]);
    }
  };
#pragma unroll
  for (int n = 0; n < NT - 2; ++n) {
    SB(); process(n); SB();
#pragma unroll
    for (int i = 0; i < 8; ++i) {
      const int k = 8 * (n + 2) + i;
      put(k); SB();
      if (k + WA < NK) issue(k + WA);
      SB();
    }
  }
  process(NT - 2);
  process(NT - 1);
  l = xg_sum(l);
  float* mrg = scr;
  if (lane < 16) {
#pragma unroll
    for (int h = 0; h < 2; ++h) {
      float* rec = mrg + (w * 2 + h) * 132;
      *reinterpret_cast<f32x4*>(rec + 4 + 8 * c) = f32x4{acc[0][h], acc[1][h], acc[2][h], acc[3][h]};
      *reinterpret_cast<f32x4*>(rec + 4 + 8 * c + 4) = f32x4{acc[4][h], acc[5][h], acc[6][h], acc[7][h]};
      if (c == h) { rec[0] = m; rec[1] = l; }
    }
  }
  __syncthreads();
  STAMP(2);
  if (t < 256) {
    const int h = t >> 7, d = t & 127;
    float M = -INFINITY;
#pragma unroll
    for (int ww = 0; ww < 8; ++ww) M = fmaxf(M, mrg[(ww * 2 + h) * 132]);
    M = q24(M);
    float L = 0.f, o = 0.f;
#pragma unroll
    for (int ww = 0; ww < 8; ++ww) {
      const float* rec = mrg + (ww * 2 + h) * 132;
      const float wt = __builtin_amdgcn_exp2f((rec[0] - M) * C2);
      L = fmaf(wt, rec[1], L); o = fmaf(wt, rec[4 + d], o);
    }
    gu64* dst = (gu64*)(p.ws + WS_GA) + (size_t)((((kvh * 2 + b) * NSA + s) * 2 + h)) * 132;
    st_gran(dst + 4 + d, tagw, o);
    if (d == 0) st_gran(dst, tagw, M);
    if (d == 1) st_gran(dst + 1, tagw, L);
  }
  STAMP(3);
}

template <class F0, class F>
__device__ __forceinline__ void qproj64(const uint16_t* x, const uint16_t* W, int col0, float* scr, float* qh, F0&& pre, F&& after) {
  const int t = threadIdx.x, lane = t & 63, w = __builtin_amdgcn_readfirstlane(t >> 6);
  const int cg = t & 7, rg = t >> 3;
  v4u wq[9]; unsigned short x0[9], x1[9];
#pragma unroll
  for (int i = 0; i < 9; ++i) { x0[i] = x[rg + 64 * i]; x1[i] = x[DM + rg + 64 * i]; }
#pragma unroll
  for (int i = 0; i < 9; ++i) wq[i] = ldnt(W + (size_t)(rg + 64 * i) * 4096 + col0 + 8 * cg);
  SB();
  pre();
  SB();
  float acc[2][8];
  zero8(acc);
#pragma unroll
  for (int i = 0; i < 9; ++i) {
    fma8(acc, bflo(x0[i]), bflo(x1[i]), wq[i]);
    SB();
    after(i);
    SB();
  }
#pragma unroll
  for (int bb = 0; bb < 2; ++bb)
#pragma unroll
    for (int jj = 0; jj < 8; ++jj) { float v = acc[bb][jj]; v += dpp<XOR8>(v); acc[bb][jj] = xg_sum(v); }
  if (lane < 8) {
#pragma unroll
    for (int bb = 0; bb < 2; ++bb) {
      *reinterpret_cast<f32x4*>(scr + (w * 2 + bb) * 64 + 8 * cg) = f32x4{acc[bb][0], acc[bb][1], acc[bb][2], acc[bb][3]};
      *reinterpret_cast<f32x4*>(scr + (w * 2 + bb) * 64 + 8 * cg + 4) = f32x4{acc[bb][4], acc[bb][5], acc[bb][6], acc[bb][7]};
    }
  }
  __syncthreads();
  if (t < 128) {
    float sacc = 0.f;
#pragma unroll
    for (int ww = 0; ww < 8; ++ww) sacc += scr[ww * 128 + t];
    qh[t] = rndbf(sacc);
  }
}

__device__ __forceinline__ void roleX(const Params& p, int idx, float* lds) {
  const int t = threadIdx.x, lane = t & 63, w = __builtin_amdgcn_readfirstlane(t >> 6);
  const __amdgpu_buffer_rsrc_t rs = __builtin_amdgcn_make_buffer_rsrc((void*)p.ws, 0, (int)WS_END, 0x00020000);
  STAMP(0);
  const int h1 = idx >> 1, dh = idx & 1;
  const int cg = t & 7, rg = t >> 3;
  const u64 E = ld_gran((gu64*)(p.ws + WS_E));
  float* scr = lds + 32768;
  float* qh = scr + 1024;
  v4u uk[8];
  qproj64(p.xB, p.wqB, 128 * h1 + 64 * dh, scr, qh, [&]() {
    if (XALL) {
#pragma unroll
      for (int i = 0; i < 8; ++i) uk[i] = ldnt(p.wuk + (size_t)(8 * rg + i) * 4096 + 128 * h1 + 64 * dh + 8 * cg);
    }
  }, [&](int i) {
    if (!XALL && i < 8) uk[i] = ldnt(p.wuk + (size_t)(8 * rg + i) * 4096 + 128 * h1 + 64 * dh + 8 * cg);
  });
  const u64 tagw = (E + 1) << 24;
  __syncthreads();
  {
    float q0[8], q1[8];
#pragma unroll
    for (int jj = 0; jj < 8; ++jj) { q0[jj] = qh[8 * cg + jj]; q1[jj] = qh[64 + 8 * cg + jj]; }
    float v0[8], v1[8];
#pragma unroll
    for (int i = 0; i < 8; ++i) {
      float s0 = 0.f, s1 = 0.f;
#pragma unroll
      for (int jj = 0; jj < 4; ++jj) {
        s0 = fmaf(q0[2 * jj], bflo(uk[i][jj]), s0); s0 = fmaf(q0[2 * jj + 1], bfhi(uk[i][jj]), s0);
        s1 = fmaf(q1[2 * jj], bflo(uk[i][jj]), s1); s1 = fmaf(q1[2 * jj + 1], bfhi(uk[i][jj]), s1);
      }
      s0 += dpp<XOR1>(s0); s0 += dpp<XOR2>(s0); s0 += dpp<XOR7>(s0);
      s1 += dpp<XOR1>(s1); s1 += dpp<XOR2>(s1); s1 += dpp<XOR7>(s1);
      v0[i] = s0; v1[i] = s1;
    }
    if (cg < 2) {
      v4u o;
      if (cg == 0) o = v4u{cvtpk(v0[0], v0[1]), cvtpk(v0[2], v0[3]), cvtpk(v0[4], v0[5]), cvtpk(v0[6], v0[7])};
      else o = v4u{cvtpk(v1[0], v1[1]), cvtpk(v1[2], v1[3]), cvtpk(v1[4], v1[5]), cvtpk(v1[6], v1[7])};
      const int off = (int)WS_QABS + (((((dh * 2 + cg) * 2 + (h1 >> 4)) * 16 + (rg >> 2)) * 64) + (rg & 3) * 16 + (h1 & 15)) * 16;
      __builtin_amdgcn_raw_buffer_store_b128(o, rs, off, 0, 16);
    }
  }
  VMW(0);
  __syncthreads();
  if (t == 0) st_gran((gu64*)(p.ws + WS_FB1) + idx, tagw, 0.f);
  STAMP(1);
  v4u uv[8], wo[10];
  const int och = t % 72, org = t / 72;
#pragma unroll
  for (int i = 0; i < 8; ++i) uv[i] = ldnt(p.wuv + (size_t)(8 * rg + i) * 4096 + 128 * h1 + 64 * dh + 8 * cg);
#pragma unroll
  for (int i = 0; i < 10; ++i) {
    const int r = org + 7 * i;
    VMW(7);
    wo[i] = ldnt(p.woB + (size_t)(128 * h1 + 64 * dh + min(r, 63)) * DM + 8 * och);
  }
#pragma unroll
  for (int i = 0; i < 10; ++i) { const int r = org + 7 * i; if (r >= 64) wo[i] = v4u{0u, 0u, 0u, 0u}; }
  VMW(0);
  STAMP(2);
  if (w == 0) prepoll((gu64*)(p.ws + WS_FB2) + lane, tagw);
  __syncthreads();
  STAMP(3);
  float* part = lds;
  float* ol = lds + 4096;
  {
    const int bb = t >> 8, sg = (t >> 6) & 3, cc = t & 63;
    v4u pv[8];
#pragma unroll
    for (int i = 0; i < 8; ++i)
      pv[i] = __builtin_amdgcn_raw_buffer_load_b128(rs, (int)(WS_PB + (((size_t)(bb * NSB + 8 * sg + i) * 32 + h1) * 512 + 8 * cc) * 2), 0, 16);
    const v2u ml = __builtin_amdgcn_raw_buffer_load_b64(rs, (int)(WS_PBML + ((size_t)(bb * NSB + (lane & 31)) * 32 + h1) * 8), 0, 16);
    const float mi = __uint_as_float(ml[0]), li = __uint_as_float(ml[1]);
    float M = mi;
    M = fmaxf(M, dpp<XOR1>(M)); M = fmaxf(M, dpp<XOR2>(M)); M = fmaxf(M, dpp<XOR7>(M)); M = fmaxf(M, dpp<XOR8>(M)); M = xg_max(M);
    const float e = __builtin_amdgcn_exp2f((mi - M) * C2);
    float L = e * li;
    L += dpp<XOR1>(L); L += dpp<XOR2>(L); L += dpp<XOR7>(L); L += dpp<XOR8>(L);
    { auto sw = __builtin_amdgcn_permlane16_swap(__float_as_uint(L), __float_as_uint(L), false, false); L = __uint_as_float(sw[0]) + __uint_as_float(sw[1]); }
    const float wl = e / L;
    float a[8];
#pragma unroll
    for (int jj = 0; jj < 8; ++jj) a[jj] = 0.f;
#pragma unroll
    for (int i = 0; i < 8; ++i) {
      const float wt = __builtin_bit_cast(float, __builtin_amdgcn_readlane(__builtin_bit_cast(int, wl), 8 * (w & 3) + i));
#pragma unroll
      for (int jj = 0; jj < 4; ++jj) { a[2 * jj] = fmaf(wt, bflo(pv[i][jj]), a[2 * jj]); a[2 * jj + 1] = fmaf(wt, bfhi(pv[i][jj]), a[2 * jj + 1]); }
    }
    *reinterpret_cast<f32x4*>(part + (bb * 4 + sg) * 512 + 8 * cc) = f32x4{a[0], a[1], a[2], a[3]};
    *reinterpret_cast<f32x4*>(part + (bb * 4 + sg) * 512 + 8 * cc + 4) = f32x4{a[4], a[5], a[6], a[7]};
  }
  __syncthreads();
  STAMP(4);
#pragma unroll
  for (int bb = 0; bb < 2; ++bb) ol[bb * 512 + t] = (part[(bb * 4) * 512 + t] + part[(bb * 4 + 1) * 512 + t]) + (part[(bb * 4 + 2) * 512 + t] + part[(bb * 4 + 3) * 512 + t]);
  __syncthreads();
  float* red = lds;
  {
    float acc[2][8];
    zero8(acc);
#pragma unroll
    for (int i = 0; i < 8; ++i) fma8(acc, ol[8 * rg + i], ol[512 + 8 * rg + i], uv[i]);
#pragma unroll
    for (int bb = 0; bb < 2; ++bb)
#pragma unroll
      for (int jj = 0; jj < 8; ++jj) { float v = acc[bb][jj]; v += dpp<XOR8>(v); acc[bb][jj] = xg_sum(v); }
    if (lane < 8) {
#pragma unroll
      for (int bb = 0; bb < 2; ++bb) {
        *reinterpret_cast<f32x4*>(red + (w * 2 + bb) * 64 + 8 * cg) = f32x4{acc[bb][0], acc[bb][1], acc[bb][2], acc[bb][3]};
        *reinterpret_cast<f32x4*>(red + (w * 2 + bb) * 64 + 8 * cg + 4) = f32x4{acc[bb][4], acc[bb][5], acc[bb][6], acc[bb][7]};
      }
    }
  }
  __syncthreads();
  float* ctx = lds + 1024;
  if (t < 128) {
    float cvv = 0.f;
#pragma unroll
    for (int ww = 0; ww < 8; ++ww) cvv += red[ww * 128 + t];
    ctx[t] = rndbf(cvv);
  }
  __syncthreads();
  float* red2 = lds + 1280;
  if (org < 7) {
    float a2[2][8];
    zero8(a2);
#pragma unroll
    for (int i = 0; i < 10; ++i) { const int r = min(org + 7 * i, 63); fma8(a2, ctx[r], ctx[64 + r], wo[i]); }
#pragma unroll
    for (int bb = 0; bb < 2; ++bb) {
      *reinterpret_cast<f32x4*>(red2 + (org * 2 + bb) * DM + 8 * och) = f32x4{a2[bb][0], a2[bb][1], a2[bb][2], a2[bb][3]};
      *reinterpret_cast<f32x4*>(red2 + (org * 2 + bb) * DM + 8 * och + 4) = f32x4{a2[bb][4], a2[bb][5], a2[bb][6], a2[bb][7]};
    }
  }
  __syncthreads();
  gu64* GOB = (gu64*)(p.ws + WS_GOB);
  for (int i = t; i < 2 * DM; i += 512) {
    float sacc = 0.f;
#pragma unroll
    for (int gq = 0; gq < 7; ++gq) sacc += red2[gq * 2 * DM + i];
    st_gran(GOB + (size_t)idx * 2 * DM + i, tagw, sacc);
  }
  STAMP(5);
  if (idx < 18) {
    const int bb = idx / 9, cbk = idx % 9;
    const int col = t & 63, sg = t >> 6;
    prepoll(GOB + ((size_t)(8 * sg + (lane & 7)) * 2 + bb) * DM + 64 * cbk, tagw);
    float v[8];
    for (unsigned spin = 0; spin < SPIN_CAP; ++spin) {
      bool ok = true;
#pragma unroll
      for (int i = 0; i < 8; ++i) { const u64 a = ld_gran(GOB + ((size_t)(8 * sg + i) * 2 + bb) * DM + 64 * cbk + col); v[i] = gval(a); ok &= gok(a, tagw); }
      if (__all(ok)) break;
      __builtin_amdgcn_s_sleep(1);
    }
    __syncthreads();
    lds[t] = ((v[0] + v[1]) + (v[2] + v[3])) + ((v[4] + v[5]) + (v[6] + v[7]));
    __syncthreads();
    if (t < 64) {
      float sacc = 0.f;
#pragma unroll
      for (int i = 0; i < 8; ++i) sacc += lds[64 * i + t];
      p.out[bb * 2 * DM + DM + 64 * cbk + t] = (uint16_t)(cvtpk(sacc, sacc) & 0xffffu);
    }
    STAMP(6);
  }
}

__device__ __forceinline__ void roleY(const Params& p, int idx, float* lds) {
  const int t = threadIdx.x, lane = t & 63, w = __builtin_amdgcn_readfirstlane(t >> 6);
  const int g = lane >> 4, c = lane & 15;
  const __amdgpu_buffer_rsrc_t rs = __builtin_amdgcn_make_buffer_rsrc((void*)p.ws, 0, (int)WS_END, 0x00020000);
  STAMP(0);
  const int kvh = idx >> 2, cq = idx & 3;
  const int b2 = idx >> 5, sp = idx & 31;
  const u64 E = ld_gran((gu64*)(p.ws + WS_E));
  unsigned char* cl = reinterpret_cast<unsigned char*>(lds);
  float* scr = lds + 32768;
  float* qh = scr + 1024;
  v4u ck[WB];
  const uint16_t* cb0 = p.ckv + ((size_t)b2 * SK + 128 * sp + 16 * w) * 512 + 8 * lane;
  qproj64(p.xA, p.wqA, 256 * kvh + 64 * cq, scr, qh, [&]() {}, [&](int i) {
    if (i < WB) ck[i] = ldnt(cb0 + i * 512);
  });
  const u64 tagw = (E + 1) << 24;
  if (t < 128) st_gran((gu64*)(p.ws + WS_GQA) + (size_t)(kvh * 2 + (t >> 6)) * 256 + 64 * cq + (t & 63), tagw, qh[t]);
  STAMP(1);
  SB();
#pragma unroll
  for (int i = 0; i < 16; ++i) {
    *reinterpret_cast<v4u*>(cl + (16 * w + i) * 1024 + ((lane ^ i) << 4)) = ck[i % WB];
    SB();
    if (i + WB < 16) ck[i % WB] = ldnt(cb0 + (i + WB) * 512);
    SB();
  }
  STAMP(2);
  const int ht = w >> 2, kq = w & 3;
  if (w == 0) prepoll((gu64*)(p.ws + WS_FB1) + lane, tagw);
  __syncthreads();
  STAMP(3);
  v4u* ql = reinterpret_cast<v4u*>(scr);
  {
    v4u qa[2][4];
#pragma unroll
    for (int d2 = 0; d2 < 2; ++d2)
#pragma unroll
      for (int i = 0; i < 4; ++i)
        qa[d2][i] = __builtin_amdgcn_raw_buffer_load_b128(rs, (int)WS_QABS + ((d2 * 2 + b2) * 2048 + i * 512 + t) * 16, 0, 16);
#pragma unroll
    for (int i = 0; i < 4; ++i) {
      v4u o;
#pragma unroll
      for (int jj = 0; jj < 4; ++jj) o[jj] = cvtpk(bflo(qa[0][i][jj]) + bflo(qa[1][i][jj]), bfhi(qa[0][i][jj]) + bfhi(qa[1][i][jj]));
      ql[i * 512 + t] = o;
    }
  }
  __syncthreads();
  STAMP(4);
  float* wmax = scr;
  float* wsum = scr + 128;
  unsigned* pl = reinterpret_cast<unsigned*>(scr + 256);
  {
    f32x4 s0 = {0.f, 0.f, 0.f, 0.f}, s1 = {0.f, 0.f, 0.f, 0.f};
#pragma unroll
    for (int i = 0; i < 16; ++i) {
      const int ch = 4 * i + g;
      const int so = ((ch & ~15) | ((ch ^ c) & 15)) << 4;
      const v4u k0 = *reinterpret_cast<const v4u*>(cl + (32 * kq + c) * 1024 + so);
      const v4u k1 = *reinterpret_cast<const v4u*>(cl + (32 * kq + 16 + c) * 1024 + so);
      const v4u qf = ql[(ht * 16 + i) * 64 + lane];
      s0 = mfma16(k0, qf, s0); s1 = mfma16(k1, qf, s1);
    }
    float m0 = xg_max(fmaxf(fmaxf(fmaxf(s0[0], s0[1]), fmaxf(s0[2], s0[3])), fmaxf(fmaxf(s1[0], s1[1]), fmaxf(s1[2], s1[3]))));
    __syncthreads();
    if (lane < 16) wmax[kq * 32 + 16 * ht + c] = m0;
    __syncthreads();
#pragma unroll
    for (int q = 0; q < 4; ++q) m0 = fmaxf(m0, wmax[q * 32 + 16 * ht + c]);
    v4u pf;
    pf[0] = cvtpk(__builtin_amdgcn_exp2f((s0[0] - m0) * C2), __builtin_amdgcn_exp2f((s0[1] - m0) * C2));
    pf[1] = cvtpk(__builtin_amdgcn_exp2f((s0[2] - m0) * C2), __builtin_amdgcn_exp2f((s0[3] - m0) * C2));
    pf[2] = cvtpk(__builtin_amdgcn_exp2f((s1[0] - m0) * C2), __builtin_amdgcn_exp2f((s1[1] - m0) * C2));
    pf[3] = cvtpk(__builtin_amdgcn_exp2f((s1[2] - m0) * C2), __builtin_amdgcn_exp2f((s1[3] - m0) * C2));
    float l0 = 0.f;
#pragma unroll
    for (int i = 0; i < 4; ++i) l0 += bflo(pf[i]) + bfhi(pf[i]);
    l0 = xg_sum(l0);
    if (lane < 16) wsum[kq * 32 + 16 * ht + c] = l0;
    *reinterpret_cast<v4u*>(pl + ((ht * 4 + kq) * 64 + lane) * 4) = pf;
  }
  __syncthreads();
  {
    const int cb = w & 3;
    f32x4 acc[8];
#pragma unroll
    for (int jj = 0; jj < 8; ++jj) acc[jj] = f32x4{0.f, 0.f, 0.f, 0.f};
#pragma unroll
    for (int kg = 0; kg < 4; ++kg) {
      const v4u pf = *reinterpret_cast<const v4u*>(pl + ((ht * 4 + kg) * 64 + lane) * 4);
      v4u cv[8];
#pragma unroll
      for (int i = 0; i < 8; ++i) {
        const int key = 32 * kg + 16 * (i >> 2) + 4 * g + (i & 3);
        cv[i] = *reinterpret_cast<const v4u*>(cl + key * 1024 + ((16 * cb + (c ^ (key & 15))) << 4));
      }
#pragma unroll
      for (int jj = 0; jj < 8; ++jj) {
        v4u vf;
#pragma unroll
        for (int i = 0; i < 4; ++i) vf[i] = __builtin_amdgcn_perm(cv[2 * i + 1][jj >> 1], cv[2 * i][jj >> 1], (jj & 1) ? 0x07060302u : 0x05040100u);
        acc[jj] = mfma16(pf, vf, acc[jj]);
      }
    }
    const int off = (int)(WS_PB + (((size_t)(b2 * NSB + sp) * 32 + 16 * ht + 4 * g) * 512 + 128 * cb + 8 * c) * 2);
#pragma unroll
    for (int r = 0; r < 4; ++r) {
      v4u o = {cvtpk(acc[0][r], acc[1][r]), cvtpk(acc[2][r], acc[3][r]), cvtpk(acc[4][r], acc[5][r]), cvtpk(acc[6][r], acc[7][r])};
      __builtin_amdgcn_raw_buffer_store_b128(o, rs, off + r * 1024, 0, 16);
    }
    if (t < 32) {
      float L = 0.f, M = -INFINITY;
#pragma unroll
      for (int q = 0; q < 4; ++q) { L += wsum[q * 32 + t]; M = fmaxf(M, wmax[q * 32 + t]); }
      __builtin_amdgcn_raw_buffer_store_b64(v2u{__float_as_uint(M), __float_as_uint(L)}, rs, (int)(WS_PBML + ((size_t)(b2 * NSB + sp) * 32 + t) * 8), 0, 16);
    }
  }
  VMW(0);
  __syncthreads();
  if (t == 0) st_gran((gu64*)(p.ws + WS_FB2) + idx, tagw, 0.f);
  STAMP(5);
  const int och = t % 18, org = t / 18;
  v4u wo[10];
#pragma unroll
  for (int i = 0; i < 10; ++i) wo[i] = ldnt(p.woA + (size_t)(256 * kvh + min(org + 28 * i, 255)) * DM + 144 * cq + 8 * och);
#pragma unroll
  for (int i = 0; i < 10; ++i) { if (org + 28 * i >= 256) wo[i] = v4u{0u, 0u, 0u, 0u}; }
  VMW(0);
  STAMP(6);
  float* os = scr;
  {
    const int bb = t >> 8, h = (t >> 7) & 1, d = t & 127;
    gu64* src = (gu64*)(p.ws + WS_GA) + (size_t)(((kvh * 2 + bb) * NSA) * 2 + h) * 132;
    prepoll(src + (lane & 3) * 264 + 4 + (d & 64), tagw);
    float Ms[NSA], Ls[NSA], Os[NSA];
    for (unsigned spin = 0; spin < SPIN_CAP; ++spin) {
      bool ok = true;
#pragma unroll
      for (int ss = 0; ss < NSA; ++ss) {
        const u64 a = ld_gran(src + ss * 264), bq = ld_gran(src + ss * 264 + 1), cq2 = ld_gran(src + ss * 264 + 4 + d);
        Ms[ss] = gval(a); Ls[ss] = gval(bq); Os[ss] = gval(cq2);
        ok &= gok(a, tagw) && gok(bq, tagw) && gok(cq2, tagw);
      }
      if (__all(ok)) break;
      __builtin_amdgcn_s_sleep(1);
    }
    float M = -INFINITY;
#pragma unroll
    for (int ss = 0; ss < NSA; ++ss) M = fmaxf(M, Ms[ss]);
    float L = 0.f, o = 0.f;
#pragma unroll
    for (int ss = 0; ss < NSA; ++ss) { const float wt = __builtin_amdgcn_exp2f((Ms[ss] - M) * C2); L = fmaf(wt, Ls[ss], L); o = fmaf(wt, Os[ss], o); }
    os[t] = rndbf(o / L);
  }
  __syncthreads();
  STAMP(7);
  float* red = lds;
  if (org < 28) {
    float a2[2][8];
    zero8(a2);
#pragma unroll
    for (int i = 0; i < 10; ++i) { const int r = min(org + 28 * i, 255); fma8(a2, os[r], os[256 + r], wo[i]); }
#pragma unroll
    for (int bb = 0; bb < 2; ++bb) {
      *reinterpret_cast<f32x4*>(red + org * 288 + bb * 144 + 8 * och) = f32x4{a2[bb][0], a2[bb][1], a2[bb][2], a2[bb][3]};
      *reinterpret_cast<f32x4*>(red + org * 288 + bb * 144 + 8 * och + 4) = f32x4{a2[bb][4], a2[bb][5], a2[bb][6], a2[bb][7]};
    }
  }
  __syncthreads();
  gu64* GO = (gu64*)(p.ws + WS_GOA) + (size_t)cq * 16 * 288;
  if (t < 288) {
    float s4[4] = {0.f, 0.f, 0.f, 0.f};
#pragma unroll
    for (int i = 0; i < 28; ++i) s4[i & 3] += red[i * 288 + t];
    st_gran(GO + kvh * 288 + t, tagw, (s4[0] + s4[1]) + (s4[2] + s4[3]));
  }
  STAMP(8);
  if (kvh < 4) {
    float* red2 = scr + 1024;
    if (t < 288) {
      const int q4 = t / 72, col = 72 * kvh + t % 72;
      float v[4];
      for (unsigned spin = 0; spin < SPIN_CAP; ++spin) {
        bool ok = true;
#pragma unroll
        for (int i = 0; i < 4; ++i) { const u64 a = ld_gran(GO + (4 * q4 + i) * 288 + col); v[i] = gval(a); ok &= gok(a, tagw); }
        if (__all(ok)) break;
        __builtin_amdgcn_s_sleep(1);
      }
      red2[t] = (v[0] + v[1]) + (v[2] + v[3]);
    }
    __syncthreads();
    if (t < 72) {
      const float sacc = (red2[t] + red2[72 + t]) + (red2[144 + t] + red2[216 + t]);
      const int col = 72 * kvh + t;
      p.out[(col / 144) * 2 * DM + 144 * cq + (col % 144)] = (uint16_t)(cvtpk(sacc, sacc) & 0xffffu);
    }
    if (idx == 0 && t == 0) __hip_atomic_store((gu64*)(p.ws + WS_E), E + 1, RLX_AGENT);
    STAMP(9);
  }
}

__global__ void __launch_bounds__(512, 2) fused(Params p) {
  __shared__ __attribute__((aligned(16))) float lds[32768 + 8192];
  if (blockIdx.x < 128) roleA(p, blockIdx.x, lds);
  else if (blockIdx.x < 192) roleX(p, blockIdx.x - 128, lds);
  else roleY(p, blockIdx.x - 192, lds);
}

extern "C" void kernel_launch(void* const* d_in, const int* in_sizes, int n_in, void* d_out, int out_size, void* d_ws, size_t ws_size,
                              hipStream_t stream) {
  Params p;
  p.xA = (const uint16_t*)d_in[0]; p.kA = (const uint16_t*)d_in[1]; p.vA = (const uint16_t*)d_in[2];
  p.wqA = (const uint16_t*)d_in[3]; p.woA = (const uint16_t*)d_in[4];
  p.xB = (const uint16_t*)d_in[5]; p.ckv = (const uint16_t*)d_in[6]; p.wqB = (const uint16_t*)d_in[7];
  p.wuk = (const uint16_t*)d_in[8]; p.wuv = (const uint16_t*)d_in[9]; p.woB = (const uint16_t*)d_in[10];
  p.out = (uint16_t*)d_out; p.ws = (unsigned char*)d_ws;
  fused<<<256, 512, 0, stream>>>(p);
}
```

```cpp
#include <hip/hip_runtime.h>
#include <stdint.h>
#include <type_traits>

typedef unsigned v4u __attribute__((ext_vector_type(4)));
typedef unsigned v2u __attribute__((ext_vector_type(2)));
typedef float f32x4 __attribute__((ext_vector_type(4)));
typedef float f32x2 __attribute__((ext_vector_type(2)));
typedef short bf16x8 __attribute__((ext_vector_type(8)));
typedef short bf16x4 __attribute__((ext_vector_type(4)));
typedef __bf16 bf16x2 __attribute__((ext_vector_type(2)));
typedef unsigned long long u64;
#define GAS __attribute__((address_space(1)))
typedef GAS u64 gu64;
#define RLX_AGENT __ATOMIC_RELAXED, __HIP_MEMORY_SCOPE_AGENT

constexpr int DM = 576;
constexpr int SK = 4096;
constexpr int NSA = 4;
constexpr int NSB = 32;
constexpr float LOG2E = 1.4426950408889634f;
constexpr float SCALE = 0.08838834764831845f;
constexpr float C2 = SCALE * LOG2E;

#define WA 12
#define KQ 12
#define WB 8
#define ADELAY 0
#define XDELAY 0
#define YDELAY 0
#define MAPX 1
#define MAPY 1
#define PBL 1

constexpr size_t WS_E = 0;
constexpr size_t WS_FB1 = 4096;
constexpr size_t WS_FB2 = WS_FB1 + 4096;
constexpr size_t WS_GQA = WS_FB2 + 4096;
constexpr size_t WS_GA = WS_GQA + (size_t)16 * 2 * 256 * 8;
constexpr size_t WS_GOA = WS_GA + (size_t)16 * 2 * NSA * 2 * 132 * 8;
constexpr size_t WS_GOB = WS_GOA + (size_t)4 * 16 * 288 * 8;
constexpr size_t WS_QABS = WS_GOB + (size_t)64 * 2 * DM * 8;
constexpr size_t WS_PB = WS_QABS + (size_t)2 * 2 * 32 * 512 * 2;
constexpr size_t WS_PBML = WS_PB + (size_t)2 * NSB * 32 * 512 * 2;
constexpr size_t WS_END = WS_PBML + (size_t)2 * NSB * 32 * 2 * 4;
constexpr size_t WS_DBG = 64u << 20;

#define STAMP(i) do {} while (0)
#define SB() __builtin_amdgcn_sched_barrier(0)
#define VMW(n) asm volatile("s_waitcnt vmcnt(" #n ")" ::: "memory")

__device__ __forceinline__ float bflo(unsigned u) { return __uint_as_float(u << 16); }
__device__ __forceinline__ float bfhi(unsigned u) { return __uint_as_float(u & 0xffff0000u); }
__device__ __forceinline__ unsigned cvtpk(float lo, float hi) {
  const bf16x2 r = __builtin_convertvector(f32x2{lo, hi}, bf16x2); return __builtin_bit_cast(unsigned, r);
}
__device__ __forceinline__ float rndbf(float x) { return bflo(cvtpk(x, x)); }
__device__ __forceinline__ f32x4 mfma16(v4u a, v4u b, f32x4 c) {
  return __builtin_amdgcn_mfma_f32_16x16x32_bf16(__builtin_bit_cast(bf16x8, a), __builtin_bit_cast(bf16x8, b), c, 0, 0, 0);
}
__device__ __forceinline__ f32x4 mfma16k16(v2u a, v2u b, f32x4 c) {
  return __builtin_amdgcn_mfma_f32_16x16x16bf16_1k(__builtin_bit_cast(bf16x4, a), __builtin_bit_cast(bf16x4, b), c, 0, 0, 0);
}
__device__ __forceinline__ v4u ldnt(const void* p) { return __builtin_nontemporal_load(reinterpret_cast<const v4u*>(p)); }
__device__ __forceinline__ v4u ld16(const void* p) { return *reinterpret_cast<const v4u*>(p); }
__device__ __forceinline__ float xg_max(float x) {
  auto s = __builtin_amdgcn_permlane16_swap(__float_as_uint(x), __float_as_uint(x), false, false);
  x = fmaxf(__uint_as_float(s[0]), __uint_as_float(s[1]));
  auto t = __builtin_amdgcn_permlane32_swap(__float_as_uint(x), __float_as_uint(x), false, false);
  return fmaxf(__uint_as_float(t[0]), __uint_as_float(t[1]));
}
__device__ __forceinline__ float xg_sum(float x) {
  auto s = __builtin_amdgcn_permlane16_swap(__float_as_uint(x), __float_as_uint(x), false, false);
  x = __uint_as_float(s[0]) + __uint_as_float(s[1]);
  auto t = __builtin_amdgcn_permlane32_swap(__float_as_uint(x), __float_as_uint(x), false, false);
  return __uint_as_float(t[0]) + __uint_as_float(t[1]);
}
template <int CTRL> __device__ __forceinline__ float dpp(float x) {
  return __builtin_bit_cast(float, __builtin_amdgcn_mov_dpp(__builtin_bit_cast(int, x), CTRL, 0xf, 0xf, true));
}
constexpr int XOR1 = 0xB1, XOR2 = 0x4E, XOR7 = 0x141, XOR8 = 0x128;

__device__ __forceinline__ float q24(float v) { unsigned u = __float_as_uint(v); u += 0x7Fu + ((u >> 8) & 1u); return __uint_as_float(u & 0xffffff00u); }
__device__ __forceinline__ void st_gran(gu64* g, u64 tagw, float v) {
  unsigned u = __float_as_uint(v); u += 0x7Fu + ((u >> 8) & 1u);
  __hip_atomic_store(g, tagw | (u64)(u >> 8), RLX_AGENT);
}
__device__ __forceinline__ u64 ld_gran(gu64* g) { return __hip_atomic_load(g, RLX_AGENT); }
__device__ __forceinline__ float gval(u64 g) { return __uint_as_float((unsigned)g << 8); }
__device__ __forceinline__ bool gok(u64 g, u64 tagw) { return ((g ^ tagw) >> 24) == 0; }
constexpr unsigned SPIN_CAP = 1u << 22;
constexpr unsigned SENT = 0xFFFFFFFFu;
__device__ __forceinline__ unsigned nos(unsigned x) { return x < 0xFFFFFFFEu ? x : 0xFFFFFFFEu; }
__device__ __forceinline__ unsigned umax4(v4u v) { return max(max(v[0], v[1]), max(v[2], v[3])); }
__device__ __forceinline__ void lbar() { asm volatile("s_waitcnt lgkmcnt(0)\n\ts_barrier" ::: "memory"); }
typedef short s16x4 __attribute__((ext_vector_type(4)));
__device__ __forceinline__ v2u trrd(const unsigned char* a) {
  return __builtin_bit_cast(v2u, __builtin_amdgcn_ds_read_tr16_b64_v4i16((__attribute__((address_space(3))) s16x4*)(a)));
}
__device__ __forceinline__ v4u trfrag(const unsigned char* a0, const unsigned char* a1) {
  const v2u x = trrd(a0), y = trrd(a1); return v4u{x[0], x[1], y[0], y[1]};
}
__device__ __forceinline__ float sel4(int g, float a, float b, float c, float d) { return g < 2 ? (g == 0 ? a : b) : (g == 2 ? c : d); }
__device__ __forceinline__ int pbo(int b, int h, int w, int e, int sp, int g) { return (int)WS_PB + ((((b * 32 + h) * 8 + w) * 2 + e) * 32 + sp) * 64 + g * 16; }
__device__ __forceinline__ void prepoll(gu64* g, u64 tagw) {
  for (unsigned spin = 0; spin < SPIN_CAP; ++spin) {
    if (__all(gok(ld_gran(g), tagw))) break;
    __builtin_amdgcn_s_sleep(2);
  }
}
__device__ __forceinline__ void fma8(float (&acc)[2][8], float a0, float a1, v4u w) {
#pragma unroll
  for (int jj = 0; jj < 4; ++jj) {
    acc[0][2 * jj] = fmaf(a0, bflo(w[jj]), acc[0][2 * jj]); acc[0][2 * jj + 1] = fmaf(a0, bfhi(w[jj]), acc[0][2 * jj + 1]);
    acc[1][2 * jj] = fmaf(a1, bflo(w[jj]), acc[1][2 * jj]); acc[1][2 * jj + 1] = fmaf(a1, bfhi(w[jj]), acc[1][2 * jj + 1]);
  }
}
__device__ __forceinline__ void zero8(float (&acc)[2][8]) {
#pragma unroll
  for (int bb = 0; bb < 2; ++bb)
#pragma unroll
    for (int jj = 0; jj < 8; ++jj) acc[bb][jj] = 0.f;
}

struct Params {
  const uint16_t *xA, *kA, *vA, *wqA, *woA, *xB, *ckv, *wqB, *wuk, *wuv, *woB;
  uint16_t* out;
  unsigned char* ws;
};

__device__ __forceinline__ void roleA(const Params& p, int bid, float* lds) {
  const int t = threadIdx.x, lane = t & 63, w = __builtin_amdgcn_readfirstlane(t >> 6);
  const int kvh = bid >> 3, b = (bid >> 2) & 1, s = bid & 3, pair = b * 16 + kvh;
  constexpr int NT = 8, NK = 8 * NT;
  const int g = lane >> 4, c = lane & 15;
  const uint16_t* Kb = p.kA + ((size_t)pair * SK + 16 * (64 * s + w)) * 128 + 8 * lane;
  const uint16_t* Vb = p.vA + ((size_t)pair * SK + 16 * (64 * s + w)) * 128 + 8 * lane;
  STAMP(0);
  const u64 E = ld_gran((gu64*)(p.ws + WS_E));
  if (ADELAY > 0) {
    const u64 tt0 = __builtin_amdgcn_s_memrealtime();
    while (__builtin_amdgcn_s_memrealtime() - tt0 < (u64)ADELAY) __builtin_amdgcn_s_sleep(4);
  }
  unsigned char* ring = reinterpret_cast<unsigned char*>(lds) + w * 16384;
  v4u r[WA];
  auto issue = [&](int k) {
    const int n = k >> 3, i = k & 7;
    r[k % WA] = ldnt((i < 4 ? Kb : Vb) + (size_t)n * (8 * 16 * 128) + (i & 3) * 512);
  };
  auto put = [&](int k) {
    const int n = k >> 3, i = k & 7, row = 4 * (i & 3) + g;
    const int pos = i < 4 ? row * 256 + ((c ^ row) << 4) : 4096 + row * 256 + (c << 4);
    *reinterpret_cast<v4u*>(ring + (n & 1) * 8192 + pos) = r[k % WA];
  };
  SB();
#pragma unroll
  for (int k = 0; k < WA; ++k) issue(k);
  SB();
  const u64 tagw = (E + 1) << 24;
  float* scr = lds + 32768;
  uint16_t* qs = reinterpret_cast<uint16_t*>(scr + 4096);
  gu64* qsrc = (gu64*)(p.ws + WS_GQA) + (size_t)pair * 0 + (size_t)(kvh * 2 + b) * 256 + (t & 255);
  u64 gq = 0;
#pragma unroll
  for (int k = 0; k < 16; ++k) {
    put(k); SB(); issue(k + WA); SB();
    if (k == KQ) { gq = ld_gran(qsrc); SB(); }
  }
  if (w >= 4) {
    for (unsigned spin = 0; spin < SPIN_CAP; ++spin) {
      if (__all(gok(gq, tagw))) break;
      __builtin_amdgcn_s_sleep(1);
      gq = ld_gran(qsrc);
    }
    qs[t & 255] = (uint16_t)((unsigned)gq >> 8);
  }
  __syncthreads();
  STAMP(1);
  v4u qf[4];
#pragma unroll
  for (int ss = 0; ss < 4; ++ss) qf[ss] = ld16(qs + (c & 1) * 128 + 32 * ss + 8 * g);
  float m = -INFINITY, l = 0.f;
  f32x4 acc[8];
#pragma unroll
  for (int jj = 0; jj < 8; ++jj) acc[jj] = f32x4{0.f, 0.f, 0.f, 0.f};
  auto process = [&](int n) {
    const unsigned char* tb = ring + (n & 1) * 8192;
    f32x4 s0 = {0.f, 0.f, 0.f, 0.f};
#pragma unroll
    for (int ss = 0; ss < 4; ++ss) s0 = mfma16(*reinterpret_cast<const v4u*>(tb + c * 256 + (((4 * ss + g) ^ c) << 4)), qf[ss], s0);
    v4u vr[4];
#pragma unroll
    for (int i = 0; i < 4; ++i) vr[i] = *reinterpret_cast<const v4u*>(tb + 4096 + (4 * g + i) * 256 + (c << 4));
    const float tm = xg_max(fmaxf(fmaxf(s0[0], s0[1]), fmaxf(s0[2], s0[3])));
    const float mn = fmaxf(m, tm);
    const float alpha = __builtin_amdgcn_exp2f((m - mn) * C2);
    m = mn;
    const v2u pf = {cvtpk(__builtin_amdgcn_exp2f((s0[0] - mn) * C2), __builtin_amdgcn_exp2f((s0[1] - mn) * C2)),
                    cvtpk(__builtin_amdgcn_exp2f((s0[2] - mn) * C2), __builtin_amdgcn_exp2f((s0[3] - mn) * C2))};
    l = fmaf(l, alpha, (bflo(pf[0]) + bfhi(pf[0])) + (bflo(pf[1]) + bfhi(pf[1])));
    const float a0 = __builtin_bit_cast(float, __builtin_amdgcn_readlane(__builtin_bit_cast(int, alpha), 0));
    const float a1 = __builtin_bit_cast(float, __builtin_amdgcn_readlane(__builtin_bit_cast(int, alpha), 1));
#pragma unroll
    for (int jj = 0; jj < 8; ++jj) {
      const unsigned sel = (jj & 1) ? 0x07060302u : 0x05040100u;
      const v2u vf = {__builtin_amdgcn_perm(vr[1][jj >> 1], vr[0][jj >> 1], sel), __builtin_amdgcn_perm(vr[3][jj >> 1], vr[2][jj >> 1], sel)};
      acc[jj][0] *= a0; acc[jj][1] *= a1;
      acc[jj] = mfma16k16(pf, vf, acc[jj]);
    }
  };
#pragma unroll
  for (int n = 0; n < NT - 2; ++n) {
    SB(); process(n); SB();
#pragma unroll
    for (int i = 0; i < 8; ++i) {
      const int k = 8 * (n + 2) + i;
      put(k); SB();
      if (k + WA < NK) issue(k + WA);
      SB();
    }
  }
  process(NT - 2);
  process(NT - 1);
  l = xg_sum(l);
  float* mrg = scr;
  if (lane < 16) {
#pragma unroll
    for (int h = 0; h < 2; ++h) {
      float* rec = mrg + (w * 2 + h) * 132;
      *reinterpret_cast<f32x4*>(rec + 4 + 8 * c) = f32x4{acc[0][h], acc[1][h], acc[2][h], acc[3][h]};
      *reinterpret_cast<f32x4*>(rec + 4 + 8 * c + 4) = f32x4{acc[4][h], acc[5][h], acc[6][h], acc[7][h]};
      if (c == h) { rec[0] = m; rec[1] = l; }
    }
  }
  __syncthreads();
  STAMP(2);
  if (t < 256) {
    const int h = t >> 7, d = t & 127;
    float M = -INFINITY;
#pragma unroll
    for (int ww = 0; ww < 8; ++ww) M = fmaxf(M, mrg[(ww * 2 + h) * 132]);
    M = q24(M);
    float L = 0.f, o = 0.f;
#pragma unroll
    for (int ww = 0; ww < 8; ++ww) {
      const float* rec = mrg + (ww * 2 + h) * 132;
      const float wt = __builtin_amdgcn_exp2f((rec[0] - M) * C2);
      L = fmaf(wt, rec[1], L); o = fmaf(wt, rec[4 + d], o);
    }
    gu64* dst = (gu64*)(p.ws + WS_GA) + (size_t)((((kvh * 2 + b) * NSA + s) * 2 + h)) * 132;
    st_gran(dst + 4 + d, tagw, o);
    if (d == 0) st_gran(dst, tagw, M);
    if (d == 1) st_gran(dst + 1, tagw, L);
  }
  STAMP(3);
}

template <class F0, class F, class F2>
__device__ __forceinline__ void qproj64(const uint16_t* x, const uint16_t* W, int col0, float* scr, float* qh, F0&& pre, F&& after, F2&& mid) {
  const int t = threadIdx.x, lane = t & 63, w = __builtin_amdgcn_readfirstlane(t >> 6);
  const int cg = t & 7, rg = t >> 3;
  v4u wq[9]; unsigned short x0[9], x1[9];
#pragma unroll
  for (int i = 0; i < 9; ++i) { x0[i] = x[rg + 64 * i]; x1[i] = x[DM + rg + 64 * i]; }
#pragma unroll
  for (int i = 0; i < 9; ++i) wq[i] = ldnt(W + (size_t)(rg + 64 * i) * 4096 + col0 + 8 * cg);
  SB();
  pre();
  SB();
  float acc[2][8];
  zero8(acc);
#pragma unroll
  for (int i = 0; i < 9; ++i) {
    fma8(acc, bflo(x0[i]), bflo(x1[i]), wq[i]);
    SB();
    after(i);
    SB();
  }
#pragma unroll
  for (int bb = 0; bb < 2; ++bb)
#pragma unroll
    for (int jj = 0; jj < 8; ++jj) { float v = acc[bb][jj]; v += dpp<XOR8>(v); acc[bb][jj] = xg_sum(v); }
  if (lane < 8) {
#pragma unroll
    for (int bb = 0; bb < 2; ++bb) {
      *reinterpret_cast<f32x4*>(scr + (w * 2 + bb) * 64 + 8 * cg) = f32x4{acc[bb][0], acc[bb][1], acc[bb][2], acc[bb][3]};
      *reinterpret_cast<f32x4*>(scr + (w * 2 + bb) * 64 + 8 * cg + 4) = f32x4{acc[bb][4], acc[bb][5], acc[bb][6], acc[bb][7]};
    }
  }
  mid();
  if (t < 128) {
    float sacc = 0.f;
#pragma unroll
    for (int ww = 0; ww < 8; ++ww) sacc += scr[ww * 128 + t];
    qh[t] = rndbf(sacc);
  }
}
__device__ __forceinline__ void wait_until(u64 t0, u64 d) {
  while (__builtin_amdgcn_s_memrealtime() - t0 < d) __builtin_amdgcn_s_sleep(4);
}

__device__ __forceinline__ void roleX(const Params& p, int idx, float* lds) {
  const int t = threadIdx.x, lane = t & 63, w = __builtin_amdgcn_readfirstlane(t >> 6);
  const int g = lane >> 4, c = lane & 15;
  const __amdgpu_buffer_rsrc_t rs = __builtin_amdgcn_make_buffer_rsrc((void*)p.ws, 0, (int)WS_END, 0x00020000);
  STAMP(0);
  const u64 tstart = XDELAY ? __builtin_amdgcn_s_memrealtime() : 0;
  const int h1 = 4 * (idx & 7) + (idx >> 4), dh = (idx >> 3) & 1;
  const int cg = t & 7, rg = t >> 3;
  const u64 E = ld_gran((gu64*)(p.ws + WS_E));
  const int qoff = (int)WS_QABS + (((((dh * 2 + (cg & 1)) * 2 + (h1 >> 4)) * 16 + (rg >> 2)) * 64) + (rg & 3) * 16 + (h1 & 15)) * 16;
  float* scr = lds + 32768;
  float* qh = scr + 1024;
  v4u uk[8];
  const u64 tagw = (E + 1) << 24;
  qproj64(p.xB, p.wqB, 128 * h1 + 64 * dh, scr, qh, [&]() {
#pragma unroll
    for (int i = 0; i < 8; ++i) uk[i] = ldnt(p.wuk + (size_t)(8 * rg + i) * 4096 + 128 * h1 + 64 * dh + 8 * cg);
  }, [&](int) {}, [&]() {
    lbar();
  });
  lbar();
  {
    float q0[8], q1[8];
#pragma unroll
    for (int jj = 0; jj < 8; ++jj) { q0[jj] = qh[8 * cg + jj]; q1[jj] = qh[64 + 8 * cg + jj]; }
    float v0[8], v1[8];
#pragma unroll
    for (int i = 0; i < 8; ++i) {
      float s0 = 0.f, s1 = 0.f;
#pragma unroll
      for (int jj = 0; jj < 4; ++jj) {
        s0 = fmaf(q0[2 * jj], bflo(uk[i][jj]), s0); s0 = fmaf(q0[2 * jj + 1], bfhi(uk[i][jj]), s0);
        s1 = fmaf(q1[2 * jj], bflo(uk[i][jj]), s1); s1 = fmaf(q1[2 * jj + 1], bfhi(uk[i][jj]), s1);
      }
      s0 += dpp<XOR1>(s0); s0 += dpp<XOR2>(s0); s0 += dpp<XOR7>(s0);
      s1 += dpp<XOR1>(s1); s1 += dpp<XOR2>(s1); s1 += dpp<XOR7>(s1);
      v0[i] = s0; v1[i] = s1;
    }
    if (cg < 2) {
      v4u o;
      if (cg == 0) o = v4u{cvtpk(v0[0], v0[1]), cvtpk(v0[2], v0[3]), cvtpk(v0[4], v0[5]), cvtpk(v0[6], v0[7])};
      else o = v4u{cvtpk(v1[0], v1[1]), cvtpk(v1[2], v1[3]), cvtpk(v1[4], v1[5]), cvtpk(v1[6], v1[7])};
      __builtin_amdgcn_raw_buffer_store_b128(o, rs, qoff, 0, 16);
    }
  }
  VMW(0);
  lbar();
  if (t < 8) st_gran((gu64*)(p.ws + WS_FB1) + t * 64 + idx, tagw, 0.f);
  STAMP(1);
  if (XDELAY) wait_until(tstart, XDELAY);
  unsigned char* uvim = reinterpret_cast<unsigned char*>(lds);
  unsigned char* woim = reinterpret_cast<unsigned char*>(lds) + 65536;
  {
    v4u uv[8], wo[9];
#pragma unroll
    for (int i = 0; i < 8; ++i) uv[i] = ldnt(p.wuv + (size_t)(8 * rg + i) * 4096 + 128 * h1 + 64 * dh + 8 * cg);
#pragma unroll
    for (int i = 0; i < 9; ++i) {
      if (i < 8) { *reinterpret_cast<v4u*>(uvim + (8 * rg + i) * 128 + cg * 16) = uv[i]; }
      SB();
      wo[i] = ldnt(p.woB + (size_t)(128 * h1 + 64 * dh) * DM + (t + 512 * i) * 8);
      SB();
    }
#pragma unroll
    for (int i = 0; i < 9; ++i) *reinterpret_cast<v4u*>(woim + (t + 512 * i) * 16) = wo[i];
  }
  lbar();
  const int bb = w >> 2, kq4 = w & 3, tq = c >> 2, tp = c & 3;
  v4u uvf[4][4], wof[5][2];
#pragma unroll
  for (int k4 = 0; k4 < 4; ++k4) {
    const int ks2 = 4 * kq4 + k4;
    const unsigned char* a = uvim + (64 * (ks2 >> 1) + 16 * g + 8 * (ks2 & 1) + tq) * 128 + 8 * tp;
#pragma unroll
    for (int nt = 0; nt < 4; ++nt) uvf[k4][nt] = trfrag(a + 32 * nt, a + 32 * nt + 512);
  }
#pragma unroll
  for (int ks = 0; ks < 2; ++ks) {
    const unsigned char* a = woim + (32 * ks + 8 * g + tq) * 1152 + 144 * w + 8 * tp;
#pragma unroll
    for (int tt = 0; tt < 5; ++tt) wof[tt][ks] = trfrag(a + 32 * tt, a + 32 * tt + 4 * 1152);
  }
  STAMP(2);
  if (w == 0) prepoll((gu64*)(p.ws + WS_FB2) + (idx & 7) * 64 + lane, tagw);
  lbar();
  STAMP(3);
  v4u pa[2][4];
  const v2u ml = __builtin_amdgcn_raw_buffer_load_b64(rs, (int)WS_PBML + ((bb * 32 + h1) * 32 + (lane & 31)) * 8, 0, 16);
#pragma unroll
  for (int k4 = 0; k4 < 4; ++k4)
#pragma unroll
    for (int st = 0; st < 2; ++st) pa[st][k4] = __builtin_amdgcn_raw_buffer_load_b128(rs, pbo(bb, h1, 2 * kq4 + (k4 >> 1), k4 & 1, 16 * st + c, g), 0, 16);
  SB();
  float w8[2][4];
  {
    const float mi = __uint_as_float(ml[0]), li = __uint_as_float(ml[1]);
    float M = mi;
    M = fmaxf(M, dpp<XOR1>(M)); M = fmaxf(M, dpp<XOR2>(M)); M = fmaxf(M, dpp<XOR7>(M)); M = fmaxf(M, dpp<XOR8>(M)); M = xg_max(M);
    const float e = __builtin_amdgcn_exp2f((mi - M) * C2);
    float L = e * li;
    L += dpp<XOR1>(L); L += dpp<XOR2>(L); L += dpp<XOR7>(L); L += dpp<XOR8>(L);
    { auto sw = __builtin_amdgcn_permlane16_swap(__float_as_uint(L), __float_as_uint(L), false, false); L = __uint_as_float(sw[0]) + __uint_as_float(sw[1]); }
    const float wl = e / L;
#pragma unroll
    for (int st = 0; st < 2; ++st)
#pragma unroll
      for (int r = 0; r < 4; ++r) w8[st][r] = __builtin_bit_cast(float, __builtin_amdgcn_ds_bpermute((16 * st + 4 * g + r) * 4, __builtin_bit_cast(int, wl)));
  }
  float* red = lds;
  {
    f32x4 acc[2][4];
#pragma unroll
    for (int st = 0; st < 2; ++st)
#pragma unroll
      for (int nt = 0; nt < 4; ++nt) acc[st][nt] = f32x4{0.f, 0.f, 0.f, 0.f};
#pragma unroll
    for (int k4 = 0; k4 < 4; ++k4)
#pragma unroll
      for (int st = 0; st < 2; ++st)
#pragma unroll
        for (int nt = 0; nt < 4; ++nt) acc[st][nt] = mfma16(pa[st][k4], uvf[k4][nt], acc[st][nt]);
    STAMP(4);
    float cp[4];
#pragma unroll
    for (int nt = 0; nt < 4; ++nt) {
      float s0 = w8[0][0] * acc[0][nt][0], s1 = w8[1][0] * acc[1][nt][0];
#pragma unroll
      for (int r = 1; r < 4; ++r) { s0 = fmaf(w8[0][r], acc[0][nt][r], s0); s1 = fmaf(w8[1][r], acc[1][nt][r], s1); }
      cp[nt] = s0 + s1;
    }
    auto s02 = __builtin_amdgcn_permlane32_swap(__float_as_uint(cp[0]), __float_as_uint(cp[2]), false, false);
    auto s13 = __builtin_amdgcn_permlane32_swap(__float_as_uint(cp[1]), __float_as_uint(cp[3]), false, false);
    const float xx = __uint_as_float(s02[0]) + __uint_as_float(s02[1]), yy = __uint_as_float(s13[0]) + __uint_as_float(s13[1]);
    auto sxy = __builtin_amdgcn_permlane16_swap(__float_as_uint(xx), __float_as_uint(yy), false, false);
    red[w * 64 + lane] = __uint_as_float(sxy[0]) + __uint_as_float(sxy[1]);
  }
  lbar();
  gu64* GOB = (gu64*)(p.ws + WS_GOB);
  {
    v4u af[2];
#pragma unroll
    for (int ks = 0; ks < 2; ++ks) {
      const float* r0 = red + (c & 1) * 256 + 32 * ks + 8 * g;
      f32x4 lo = *reinterpret_cast<const f32x4*>(r0), hi = *reinterpret_cast<const f32x4*>(r0 + 4);
      f32x4 lo2 = *reinterpret_cast<const f32x4*>(r0 + 128), hi2 = *reinterpret_cast<const f32x4*>(r0 + 132);
      lo += *reinterpret_cast<const f32x4*>(r0 + 64); hi += *reinterpret_cast<const f32x4*>(r0 + 68);
      lo2 += *reinterpret_cast<const f32x4*>(r0 + 192); hi2 += *reinterpret_cast<const f32x4*>(r0 + 196);
      lo += lo2; hi += hi2;
      af[ks] = v4u{cvtpk(lo[0], lo[1]), cvtpk(lo[2], lo[3]), cvtpk(hi[0], hi[1]), cvtpk(hi[2], hi[3])};
    }
    f32x4 o[5];
#pragma unroll
    for (int tt = 0; tt < 5; ++tt) o[tt] = mfma16(af[0], wof[tt][0], f32x4{0.f, 0.f, 0.f, 0.f});
#pragma unroll
    for (int tt = 0; tt < 5; ++tt) o[tt] = mfma16(af[1], wof[tt][1], o[tt]);
    gu64* dst = GOB + (size_t)idx * 2 * DM + 72 * w + lane;
    st_gran(dst, tagw, sel4(g, o[0][0], o[1][0], o[2][0], o[3][0]));
    st_gran(dst + DM, tagw, sel4(g, o[0][1], o[1][1], o[2][1], o[3][1]));
    if (lane < 8) { st_gran(dst + 64, tagw, o[4][0]); st_gran(dst + 64 + DM, tagw, o[4][1]); }
  }
  STAMP(5);
  if (idx < 18) {
    const int b3 = idx / 9, cbk = idx % 9;
    float v[8];
    for (unsigned spin = 0; spin < SPIN_CAP; ++spin) {
      bool ok = true;
#pragma unroll
      for (int i = 0; i < 8; ++i) { const u64 a = ld_gran(GOB + ((size_t)(8 * w + i) * 2 + b3) * DM + 64 * cbk + lane); v[i] = gval(a); ok &= gok(a, tagw); }
      if (__all(ok)) break;
      __builtin_amdgcn_s_sleep(1);
    }
    float* fr = lds + 1024;
    fr[t] = ((v[0] + v[1]) + (v[2] + v[3])) + ((v[4] + v[5]) + (v[6] + v[7]));
    lbar();
    if (t < 64) {
      float sacc = 0.f;
#pragma unroll
      for (int i = 0; i < 8; ++i) sacc += fr[64 * i + t];
      p.out[b3 * 2 * DM + DM + 64 * cbk + t] = (uint16_t)(cvtpk(sacc, sacc) & 0xffffu);
    }
    STAMP(6);
  }
}

__device__ __forceinline__ void roleY(const Params& p, int idx, float* lds) {
  const int t = threadIdx.x, lane = t & 63, w = __builtin_amdgcn_readfirstlane(t >> 6);
  const int g = lane >> 4, c = lane & 15, tq = c >> 2, tp = c & 3;
  const __amdgpu_buffer_rsrc_t rs = __builtin_amdgcn_make_buffer_rsrc((void*)p.ws, 0, (int)WS_END, 0x00020000);
  STAMP(0);
  const u64 tstart = YDELAY ? __builtin_amdgcn_s_memrealtime() : 0;
  const int sl = (idx >> 3) + 8 * (idx & 7);
  const int kvh = sl >> 2, cq = sl & 3;
  const int b2 = idx >> 5, sp = idx & 31;
  const u64 E = ld_gran((gu64*)(p.ws + WS_E));
  unsigned char* cl = reinterpret_cast<unsigned char*>(lds);
  float* scr = lds + 32768;
  float* qh = scr + 1024;
  v4u ck[WB];
  const uint16_t* cb0 = p.ckv + ((size_t)b2 * SK + 128 * sp + 16 * w) * 512 + 8 * lane;
  const int mloff = (int)WS_PBML + ((b2 * 32 + 4 * w + g) * 32 + sp) * 8;
  qproj64(p.xA, p.wqA, 256 * kvh + 64 * cq, scr, qh, [&]() {
  }, [&](int i) {
    if (i < WB) ck[i] = ldnt(cb0 + i * 512);
  }, [&]() { lbar(); });
  const u64 tagw = (E + 1) << 24;
  if (t < 128) st_gran((gu64*)(p.ws + WS_GQA) + (size_t)(kvh * 2 + (t >> 6)) * 256 + 64 * cq + (t & 63), tagw, qh[t]);
  STAMP(1);
  SB();
  const int poff = pbo(b2, lane >> 3, w, (lane >> 2) & 1, sp, lane & 3);
#pragma unroll
  for (int i = 0; i < 16; ++i) {
    *reinterpret_cast<v4u*>(cl + (16 * w + i) * 1024 + ((lane ^ i) << 4)) = ck[i % WB];
    SB();
    if (i + WB < 16) ck[i % WB] = ldnt(cb0 + (i + WB) * 512);
    SB();
  }
  lbar();
  STAMP(2);
  const int kq = w & 3, ch = w >> 2;
  v4u kf[2][8], vt[4][4];
#pragma unroll
  for (int kt = 0; kt < 2; ++kt)
#pragma unroll
    for (int i = 0; i < 8; ++i) {
      const int chn = 4 * (8 * ch + i) + g;
      kf[kt][i] = *reinterpret_cast<const v4u*>(cl + (32 * kq + 16 * kt + c) * 1024 + (((chn & ~15) | ((chn ^ c) & 15)) << 4));
    }
#pragma unroll
  for (int ks = 0; ks < 4; ++ks)
#pragma unroll
    for (int nt = 0; nt < 4; ++nt) {
      const int chn = 8 * w + 2 * tp + (nt >> 1);
      const int k0 = 32 * ks + 8 * g + tq, k1 = k0 + 4;
      vt[ks][nt] = trfrag(cl + k0 * 1024 + (((chn & ~15) | ((chn ^ k0) & 15)) << 4) + 8 * (nt & 1),
                          cl + k1 * 1024 + (((chn & ~15) | ((chn ^ k1) & 15)) << 4) + 8 * (nt & 1));
    }
  if (w == 0) prepoll((gu64*)(p.ws + WS_FB1) + (idx & 7) * 64 + lane, tagw);
  lbar();
  STAMP(3);
  v4u* ql = reinterpret_cast<v4u*>(scr);
  {
    v4u qa[2][4];
#pragma unroll
    for (int i = 0; i < 4; ++i)
#pragma unroll
      for (int d2 = 0; d2 < 2; ++d2)
        qa[d2][i] = __builtin_amdgcn_raw_buffer_load_b128(rs, (int)WS_QABS + ((d2 * 2 + b2) * 2048 + i * 512 + t) * 16, 0, 16);
#pragma unroll
    for (int i = 0; i < 4; ++i) {
      v4u o;
#pragma unroll
      for (int jj = 0; jj < 4; ++jj) o[jj] = cvtpk(bflo(qa[0][i][jj]) + bflo(qa[1][i][jj]), bfhi(qa[0][i][jj]) + bfhi(qa[1][i][jj]));
      ql[i * 512 + t] = o;
    }
  }
  lbar();
  STAMP(4);
  float* sx = lds;
  unsigned char* pl = reinterpret_cast<unsigned char*>(lds) + 40960;
  {
    v4u qf[2][8];
#pragma unroll
    for (int i = 0; i < 8; ++i)
#pragma unroll
      for (int ht = 0; ht < 2; ++ht) qf[ht][i] = ql[(ht * 16 + 8 * ch + i) * 64 + lane];
    SB();
    f32x4 s[2][2];
#pragma unroll
    for (int kt = 0; kt < 2; ++kt)
#pragma unroll
      for (int ht = 0; ht < 2; ++ht) s[kt][ht] = f32x4{0.f, 0.f, 0.f, 0.f};
#pragma unroll
    for (int i = 0; i < 8; ++i)
#pragma unroll
      for (int ht = 0; ht < 2; ++ht)
#pragma unroll
        for (int kt = 0; kt < 2; ++kt) s[kt][ht] = mfma16(kf[kt][i], qf[ht][i], s[kt][ht]);
#pragma unroll
    for (int kt = 0; kt < 2; ++kt)
#pragma unroll
      for (int ht = 0; ht < 2; ++ht) *reinterpret_cast<f32x4*>(sx + (ch * 32 + 16 * ht + c) * 132 + 32 * kq + 16 * kt + 4 * g) = s[kt][ht];
  }
  lbar();
  STAMP(10);
  {
    const int head = 4 * w + g;
    const float* r0 = sx + head * 132 + 8 * c;
    const f32x4 a0 = *reinterpret_cast<const f32x4*>(r0) + *reinterpret_cast<const f32x4*>(r0 + 32 * 132);
    const f32x4 a1 = *reinterpret_cast<const f32x4*>(r0 + 4) + *reinterpret_cast<const f32x4*>(r0 + 32 * 132 + 4);
    float m0 = fmaxf(fmaxf(fmaxf(a0[0], a0[1]), fmaxf(a0[2], a0[3])), fmaxf(fmaxf(a1[0], a1[1]), fmaxf(a1[2], a1[3])));
    m0 = fmaxf(m0, dpp<XOR1>(m0)); m0 = fmaxf(m0, dpp<XOR2>(m0)); m0 = fmaxf(m0, dpp<XOR7>(m0)); m0 = fmaxf(m0, dpp<XOR8>(m0));
    v4u pf;
    pf[0] = cvtpk(__builtin_amdgcn_exp2f((a0[0] - m0) * C2), __builtin_amdgcn_exp2f((a0[1] - m0) * C2));
    pf[1] = cvtpk(__builtin_amdgcn_exp2f((a0[2] - m0) * C2), __builtin_amdgcn_exp2f((a0[3] - m0) * C2));
    pf[2] = cvtpk(__builtin_amdgcn_exp2f((a1[0] - m0) * C2), __builtin_amdgcn_exp2f((a1[1] - m0) * C2));
    pf[3] = cvtpk(__builtin_amdgcn_exp2f((a1[2] - m0) * C2), __builtin_amdgcn_exp2f((a1[3] - m0) * C2));
    *reinterpret_cast<v4u*>(pl + (((head >> 4) * 4 + (c >> 2)) * 64 + (c & 3) * 16 + ((head & 15) ^ c)) * 16) = pf;
    float l0 = 0.f;
#pragma unroll
    for (int i = 0; i < 4; ++i) l0 += bflo(pf[i]) + bfhi(pf[i]);
    l0 += dpp<XOR1>(l0); l0 += dpp<XOR2>(l0); l0 += dpp<XOR7>(l0); l0 += dpp<XOR8>(l0);
    if (c == 0) __builtin_amdgcn_raw_buffer_store_b64(v2u{__float_as_uint(m0), __float_as_uint(l0)}, rs, mloff, 0, 16);
  }
  lbar();
  STAMP(11);
  {
    unsigned char* tb = reinterpret_cast<unsigned char*>(lds) + 49152 + w * 4096;
    v4u pf[2][4];
#pragma unroll
    for (int ks = 0; ks < 4; ++ks)
#pragma unroll
      for (int ht = 0; ht < 2; ++ht) pf[ht][ks] = *reinterpret_cast<const v4u*>(pl + ((ht * 4 + ks) * 64 + g * 16 + (c ^ (4 * ks + g))) * 16);
    SB();
#pragma unroll
    for (int ht = 0; ht < 2; ++ht) {
      f32x4 acc[4];
#pragma unroll
      for (int nt = 0; nt < 4; ++nt) acc[nt] = f32x4{0.f, 0.f, 0.f, 0.f};
#pragma unroll
      for (int ks = 0; ks < 4; ++ks)
#pragma unroll
        for (int nt = 0; nt < 4; ++nt) acc[nt] = mfma16(vt[ks][nt], pf[ht][ks], acc[nt]);
#pragma unroll
      for (int e = 0; e < 2; ++e) {
        const v4u o = {cvtpk(acc[2 * e][0], acc[2 * e][1]), cvtpk(acc[2 * e][2], acc[2 * e][3]),
                       cvtpk(acc[2 * e + 1][0], acc[2 * e + 1][1]), cvtpk(acc[2 * e + 1][2], acc[2 * e + 1][3])};
        *reinterpret_cast<v4u*>(tb + (16 * ht + c) * 128 + (((4 * e + g) ^ (c & 7)) << 4)) = o;
      }
      asm volatile("s_waitcnt lgkmcnt(0)" ::: "memory");
#pragma unroll
      for (int i = 0; i < 2; ++i) {
        const int hh = 16 * ht + 8 * i + (lane >> 3);
        const v4u o = *reinterpret_cast<const v4u*>(tb + hh * 128 + (((lane & 7) ^ (lane >> 3)) << 4));
        __builtin_amdgcn_raw_buffer_store_b128(o, rs, poff + (2 * ht + i) * 8 * 32768, 0, 16);
      }
    }
  }
  STAMP(12);
  VMW(0);
  lbar();
  if (t < 8) st_gran((gu64*)(p.ws + WS_FB2) + t * 64 + idx, tagw, 0.f);
  STAMP(5);
  if (YDELAY) wait_until(tstart, YDELAY);
  v4u wof[9];
  unsigned char* wim = reinterpret_cast<unsigned char*>(lds) + 81920 + w * 9216;
  {
    v4u wo[9];
#pragma unroll
    for (int i = 0; i < 9; ++i) {
      const int id = lane + 64 * i;
      wo[i] = ldnt(p.woA + (size_t)(256 * kvh + 32 * w + id / 18) * DM + 144 * cq + 8 * (id % 18));
    }
#pragma unroll
    for (int i = 0; i < 9; ++i) *reinterpret_cast<v4u*>(wim + (lane + 64 * i) * 16) = wo[i];
    asm volatile("s_waitcnt lgkmcnt(0)" ::: "memory");
    const unsigned char* a = wim + (8 * g + tq) * 288 + 8 * tp;
#pragma unroll
    for (int tt = 0; tt < 9; ++tt) wof[tt] = trfrag(a + 32 * tt, a + 32 * tt + 4 * 288);
    asm volatile("s_waitcnt lgkmcnt(0)" ::: "memory");
  }
  STAMP(6);
  float* red = lds;
  {
    const int bb = lane >> 5, h = w >> 2, d = 32 * (w & 3) + (lane & 31);
    gu64* src = (gu64*)(p.ws + WS_GA) + (size_t)(((kvh * 2 + bb) * NSA) * 2 + h) * 132;
    float Os[NSA], mi, li;
    for (unsigned spin = 0; spin < SPIN_CAP; ++spin) {
      const u64 a = ld_gran(src + (lane & 3) * 264), bq = ld_gran(src + (lane & 3) * 264 + 1);
      bool ok = gok(a, tagw) && gok(bq, tagw);
      mi = gval(a); li = gval(bq);
#pragma unroll
      for (int ss = 0; ss < NSA; ++ss) { const u64 cq2 = ld_gran(src + ss * 264 + 4 + d); Os[ss] = gval(cq2); ok &= gok(cq2, tagw); }
      if (__all(ok)) break;
      __builtin_amdgcn_s_sleep(2);
    }
    STAMP(7);
    float M = fmaxf(mi, dpp<XOR1>(mi)); M = fmaxf(M, dpp<XOR2>(M));
    const float e = __builtin_amdgcn_exp2f((mi - M) * C2);
    float L = e * li; L += dpp<XOR1>(L); L += dpp<XOR2>(L);
    const float o = fmaf(dpp<0x00>(e), Os[0], dpp<0x55>(e) * Os[1]) + fmaf(dpp<0xAA>(e), Os[2], dpp<0xFF>(e) * Os[3]);
    const float ov = o / L;
    reinterpret_cast<uint16_t*>(wim)[lane] = (uint16_t)(cvtpk(ov, ov) & 0xffffu);
    asm volatile("s_waitcnt lgkmcnt(0)" ::: "memory");
    const v4u af = *reinterpret_cast<const v4u*>(wim + (c & 1) * 64 + g * 16);
    f32x4 r[9];
#pragma unroll
    for (int tt = 0; tt < 9; ++tt) r[tt] = mfma16(af, wof[tt], f32x4{0.f, 0.f, 0.f, 0.f});
#pragma unroll
    for (int b4 = 0; b4 < 2; ++b4) {
      red[w * 288 + b4 * 144 + lane] = sel4(g, r[0][b4], r[1][b4], r[2][b4], r[3][b4]);
      red[w * 288 + b4 * 144 + 64 + lane] = sel4(g, r[4][b4], r[5][b4], r[6][b4], r[7][b4]);
      if (lane < 16) red[w * 288 + b4 * 144 + 128 + lane] = r[8][b4];
    }
  }
  lbar();
  gu64* GO = (gu64*)(p.ws + WS_GOA) + (size_t)cq * 16 * 288;
  if (t < 288) {
    float s4[2] = {0.f, 0.f};
#pragma unroll
    for (int i = 0; i < 8; ++i) s4[i & 1] += red[i * 288 + t];
    st_gran(GO + kvh * 288 + t, tagw, s4[0] + s4[1]);
  }
  STAMP(8);
  if (kvh < 4) {
    float* red2 = scr + 1024;
    if (t < 288) {
      const int q4 = t / 72, col = 72 * kvh + t % 72;
      float v[4];
      for (unsigned spin = 0; spin < SPIN_CAP; ++spin) {
        bool ok = true;
#pragma unroll
        for (int i = 0; i < 4; ++i) { const u64 a = ld_gran(GO + (4 * q4 + i) * 288 + col); v[i] = gval(a); ok &= gok(a, tagw); }
        if (__all(ok)) break;
        __builtin_amdgcn_s_sleep(1);
      }
      red2[t] = (v[0] + v[1]) + (v[2] + v[3]);
    }
    lbar();
    if (t < 72) {
      const float sacc = (red2[t] + red2[72 + t]) + (red2[144 + t] + red2[216 + t]);
      const int col = 72 * kvh + t;
      p.out[(col / 144) * 2 * DM + 144 * cq + (col % 144)] = (uint16_t)(cvtpk(sacc, sacc) & 0xffffu);
    }
    if (kvh == 0 && cq == 0 && t == 0) __hip_atomic_store((gu64*)(p.ws + WS_E), E + 1, RLX_AGENT);
    STAMP(9);
  }
}

__global__ void __launch_bounds__(512, 2) fused(Params p) {
  __shared__ __attribute__((aligned(16))) float lds[32768 + 8192];
  if (blockIdx.x < 128) roleA(p, blockIdx.x, lds);
  else if (blockIdx.x < 192) roleX(p, blockIdx.x - 128, lds);
  else roleY(p, blockIdx.x - 192, lds);
}

extern "C" void kernel_launch(void* const* d_in, const int* in_sizes, int n_in, void* d_out, int out_size, void* d_ws, size_t ws_size,
                              hipStream_t stream) {
  Params p;
  p.xA = (const uint16_t*)d_in[0]; p.kA = (const uint16_t*)d_in[1]; p.vA = (const uint16_t*)d_in[2];
  p.wqA = (const uint16_t*)d_in[3]; p.woA = (const uint16_t*)d_in[4];
  p.xB = (const uint16_t*)d_in[5]; p.ckv = (const uint16_t*)d_in[6]; p.wqB = (const uint16_t*)d_in[7];
  p.wuk = (const uint16_t*)d_in[8]; p.wuv = (const uint16_t*)d_in[9]; p.woB = (const uint16_t*)d_in[10];
  p.out = (uint16_t*)d_out; p.ws = (unsigned char*)d_ws;
  fused<<<256, 512, 0, stream>>>(p);
}
```
